```python
import torch

def _softmax(x, axis=-1):
    return torch.softmax(x, dim=axis)
def _gelu(x):
    return torch.nn.functional.gelu(x, approximate="tanh")
def _silu(x):
    return torch.nn.functional.silu(x)
def _sigmoid(x):
    return torch.sigmoid(x)
def _layernorm(x, axis=-1, eps=1e-5):
    m = x.mean(dim=axis, keepdim=True)
    v = x.var(dim=axis, keepdim=True, unbiased=False)
    return (x - m) / torch.sqrt(v + eps)
def _scatter_add(tgt, idx, src, axis=0):
    return tgt.clone().index_add_(axis, idx, src)
def _segment_sum(src, seg, n):
    out = torch.zeros((n,) + src.shape[1:], dtype=src.dtype)
    return out.index_add_(0, seg, src)
def _pad(x, pw):
    flat = tuple(v for pair in reversed(pw) for v in pair)
    return torch.nn.functional.pad(x, flat)

kernel_name = 'gated_mix__ssm_conv_s4__grouped_kv_mqa__prompt'

def setup_inputs():

    _w = (lambda a: (lambda t: t.to(torch.bfloat16) if t.is_floating_point() else t)(torch.as_tensor(a)))
    def _A_setup():
        B, L, D, N = 1, 2048, 640, 128
        A_re = -(lambda _x: torch.exp(torch.as_tensor(_x)))((lambda *s: torch.randn(*s, dtype=torch.bfloat16) * 3.2)(D, N))
        A_im = torch.pi * (lambda *s: torch.randn(*s, dtype=torch.bfloat16) * 0.05)(D, N)
        B_re = (lambda *s: torch.randn(*s, dtype=torch.bfloat16) * 0.1)(D, N)
        B_im = (lambda *s: torch.randn(*s, dtype=torch.bfloat16) * 0.1)(D, N)
        C_re = (lambda *s: torch.randn(*s, dtype=torch.bfloat16) * 0.1)(D, N)
        C_im = (lambda *s: torch.randn(*s, dtype=torch.bfloat16) * 0.1)(D, N)
        log_dt = (lambda *s: torch.randn(*s, dtype=torch.bfloat16) * 0.05)(D) * 0.5 - 4.0
        Dskip = (lambda *s: torch.randn(*s, dtype=torch.bfloat16) * 0.05)(D)
        return {
            "u": _w((lambda *s: torch.randn(*s, dtype=torch.bfloat16) * 0.05)(B, L, D)),
            "A_re": _w(A_re),
            "A_im": _w(A_im),
            "B_re": _w(B_re),
            "B_im": _w(B_im),
            "C_re": _w(C_re),
            "C_im": _w(C_im),
            "log_dt": _w(log_dt),
            "Dskip": _w(Dskip),
        }
    _A_in = _A_setup()

    def _B_setup():
        B, S, D, H = 1, 2048, 640, 32
        Dh = D // H
        return {
            "x": _w((lambda *s: torch.randn(*s, dtype=torch.bfloat16) * 0.05)(B, S, D)),
            "wq": _w((lambda *s: torch.randn(*s, dtype=torch.bfloat16) * 0.8)(D, H * Dh)),
            "wk": _w((lambda *s: torch.randn(*s, dtype=torch.bfloat16) * 0.8)(D, Dh)),
            "wv": _w((lambda *s: torch.randn(*s, dtype=torch.bfloat16) * 0.05)(D, Dh)),
            "wo": _w((lambda *s: torch.randn(*s, dtype=torch.bfloat16) * 0.05)(H * Dh, D)),
        }
    _B_in = _B_setup()

    return {
        "A_u": _A_in["u"],
        "A_A_re": _A_in["A_re"],
        "A_A_im": _A_in["A_im"],
        "A_B_re": _A_in["B_re"],
        "A_B_im": _A_in["B_im"],
        "A_C_re": _A_in["C_re"],
        "A_C_im": _A_in["C_im"],
        "A_log_dt": _A_in["log_dt"],
        "A_Dskip": _A_in["Dskip"],
        "B_x": _B_in["x"],
        "B_wq": _B_in["wq"],
        "B_wk": _B_in["wk"],
        "B_wv": _B_in["wv"],
        "B_wo": _B_in["wo"],
        "gate_w": _w((lambda *s: torch.randn(*s, dtype=torch.bfloat16) * 0.05)(640) * 16.0),
    }

def reference(A_u, A_A_re, A_A_im, A_B_re, A_B_im, A_C_re, A_C_im, A_log_dt, A_Dskip, B_x, B_wq, B_wk, B_wv, B_wo, gate_w):
    def _A_ref(u, A_re, A_im, B_re, B_im, C_re, C_im, log_dt, Dskip):
        Bsz, L, D = u.shape
        dt = torch.exp(log_dt)[:, None]
        A = A_re + 1j * A_im
        Bc = B_re + 1j * B_im
        Cc = C_re + 1j * C_im
        logdA = dt * A
        dB = dt * Bc
        l = (lambda x, dt: x.to(dt))(torch.arange(L), A_re.dtype)
        P = torch.exp(logdA[:, :, None] * l[None, None, :])
        K = torch.einsum("dn,dnl,dn->dl", Cc, P, dB).real
        L2 = 2 * L
        Kp = _pad(K, ((0, 0), (0, L)))
        up = (lambda x, *d: x.permute(*d))(u, 0, 2, 1)
        up = _pad(up, ((0, 0), (0, 0), (0, L)))
        Kf = (lambda x, axes=(-2,-1): torch.fft.rfft2(x.float(), dim=axes))(Kp[:, None, :], axes=(-2, -1))
        Uf = (lambda x, axes=(-2,-1): torch.fft.rfft2(x.float(), dim=axes))(up[:, :, None, :], axes=(-2, -1))
        Yf = Uf * Kf[None, :, :, :]
        y = (lambda x, s, axes=(-2,-1): torch.fft.irfft2(x, s=s, dim=axes).to(torch.bfloat16))(Yf, (1, L2), axes=(-2, -1))[:, :, 0, :L]
        y = (lambda x, *d: x.permute(*d))(y, 0, 2, 1) + u * Dskip[None, None, :]
        return y

    def _B_ref(x, wq, wk, wv, wo):
        B, S, D = x.shape
        H = 32
        Dh = D // H
        q = (lambda x, *d: x.permute(*d))((x @ wq).reshape(B, S, H, Dh), 0, 2, 1, 3)
        k = x @ wk
        v = x @ wv
        scores = torch.einsum("bhqd,bkd->bhqk", q, k) * (Dh ** -0.5)
        p = _softmax(scores, axis=-1)
        o = torch.einsum("bhqk,bkd->bhqd", p, v)
        return (lambda x, *d: x.permute(*d))(o, 0, 2, 1, 3).reshape(B, S, H * Dh) @ wo

    x = A_u
    g = _sigmoid((x * gate_w).sum(-1, keepdims=True))
    a = _A_ref(x, A_A_re, A_A_im, A_B_re, A_B_im, A_C_re, A_C_im, A_log_dt, A_Dskip)
    b = _B_ref(x, B_wq, B_wk, B_wv, B_wo)
    return g * a + (1 - g) * b
```

```cpp
#define CONVSLEEP 1
#define NOPF
#include <hip/hip_runtime.h>
#include <hip/hip_bf16.h>
#include <stdint.h>

typedef unsigned short u16;
typedef __attribute__((ext_vector_type(8))) __bf16 bf16x8;
typedef __attribute__((ext_vector_type(2))) __bf16 bf16x2;
typedef __attribute__((ext_vector_type(4))) short s16x4;
typedef __attribute__((ext_vector_type(2))) float f32x2;
typedef __attribute__((ext_vector_type(4))) float f32x4;
typedef __attribute__((ext_vector_type(16))) float f32x16;
typedef __attribute__((ext_vector_type(4))) unsigned u32x4;
typedef __attribute__((ext_vector_type(2))) unsigned u32x2;
#define LAS __attribute__((address_space(3)))
typedef LAS char* lds_ptr;
typedef LAS unsigned* lds_u32;

#define ATTN_ASM \
  "s_cmp_lg_u32 %[kh], 0\n" \
  "s_cbranch_scc1 L_b%=\n" \
  "s_setprio 2\n" \
  "v_mov_b32 v132, 0\n" \
  "v_mov_b32 v133, 0\n" \
  "v_mov_b32 v134, 0\n" \
  "v_mov_b32 v135, 0\n" \
  "v_mov_b32 v163, 1\n" \
  "ds_write_b32 %[ad], v163\n" \
  "s_mov_b32 m0, %[dst]\n" \
  "s_nop 0\n" \
  "global_load_lds_dwordx4 %[go], %[src] sc1\n" \
  "global_load_lds_dwordx4 %[go], %[src] offset:320 sc1\n" \
  "v_add_u32 %[go], 10752, %[go]\n" \
  "ds_read_b64 v[160:161], %[ab] offset:23552\n" \
  "ds_read_b128 v[152:155], %[ak] offset:21504\n" \
  "ds_read_b128 v[148:151], %[av] offset:24064\n" \
  "ds_read_b128 v[136:139], %[av] offset:24064\n" \
  "ds_read_b128 v[140:143], %[av] offset:24768\n" \
  "ds_read_b128 v[156:159], %[ak] offset:22528\n" \
  "s_waitcnt lgkmcnt(5)\n" \
  "v_mfma_f32_32x32x4_2b_bf16 v[64:95], v[160:161], %[qb], 0 cbsz:1 abid:0\n" \
  "s_nop 15\n" \
  "s_nop 2\n" \
  "s_waitcnt lgkmcnt(4)\n" \
  "v_mfma_f32_32x32x16_bf16 v[64:79], v[152:155], %[qa0], v[64:79]\n" \
  "v_mfma_f32_32x32x16_bf16 v[80:95], v[152:155], %[qa1], v[80:95]\n" \
  "s_waitcnt lgkmcnt(0)\n" \
  "s_nop 7\n" \
  "s_mov_b32 %[cnt], 4\n" \
  "s_mov_b32 %[nd], 1\n" \
  "s_movk_i32 %[inc], 10752\n" \
  "L_loopa%=:\n" \
  "v_exp_f32 v64, v64\n" \
  "v_exp_f32 v65, v65\n" \
  "v_mfma_f32_32x32x16_bf16 %[ob], v[148:151], v[132:135], %[ob]\n" \
  "ds_read_b128 v[144:147], %[av] offset:25472\n" \
  "ds_read_b128 v[148:151], %[av] offset:26176\n" \
  "v_exp_f32 v66, v66\n" \
  "v_exp_f32 v67, v67\n" \
  "v_exp_f32 v68, v68\n" \
  "v_mfma_f32_32x32x4_2b_bf16 v[96:127], v[160:161], %[qb], 0 cbsz:1 abid:1\n" \
  "v_exp_f32 v69, v69\n" \
  "v_exp_f32 v70, v70\n" \
  "v_exp_f32 v71, v71\n" \
  "v_cvt_pk_bf16_f32 v128, v64, v65\n" \
  "v_cvt_pk_bf16_f32 v129, v66, v67\n" \
  "v_cvt_pk_bf16_f32 v130, v68, v69\n" \
  "v_cvt_pk_bf16_f32 v131, v70, v71\n" \
  "v_exp_f32 v72, v72\n" \
  "v_exp_f32 v73, v73\n" \
  "v_exp_f32 v74, v74\n" \
  "s_waitcnt lgkmcnt(2)\n" \
  "v_mfma_f32_32x32x16_bf16 %[oa], v[136:139], v[128:131], %[oa]\n" \
  "v_exp_f32 v75, v75\n" \
  "v_exp_f32 v76, v76\n" \
  "v_exp_f32 v77, v77\n" \
  "v_exp_f32 v78, v78\n" \
  "v_exp_f32 v79, v79\n" \
  "ds_read_b32 v162, %[af]\n" \
  "v_cvt_pk_bf16_f32 v132, v72, v73\n" \
  "v_cvt_pk_bf16_f32 v133, v74, v75\n" \
  "v_cvt_pk_bf16_f32 v134, v76, v77\n" \
  "v_cvt_pk_bf16_f32 v135, v78, v79\n" \
  "v_mfma_f32_32x32x16_bf16 v[96:111], v[156:159], %[qa0], v[96:111]\n" \
  "v_exp_f32 v80, v80\n" \
  "v_exp_f32 v81, v81\n" \
  "v_exp_f32 v82, v82\n" \
  "v_exp_f32 v83, v83\n" \
  "v_mfma_f32_32x32x16_bf16 %[oa], v[140:143], v[132:135], %[oa]\n" \
  "s_setprio 0\n" \
  "v_exp_f32 v84, v84\n" \
  "v_exp_f32 v85, v85\n" \
  "v_exp_f32 v86, v86\n" \
  "v_exp_f32 v87, v87\n" \
  "s_waitcnt lgkmcnt(0)\n" \
  "v_cmp_gt_u32 vcc, %[nd], v162\n" \
  "s_add_u32 m0, %[dst], 10752\n" \
  "s_cbranch_vccnz L_polla0_%=\n" \
  "L_oka0_%=:\n" \
  "global_load_lds_dwordx4 %[go], %[src] sc1\n" \
  "global_load_lds_dwordx4 %[go], %[src] offset:320 sc1\n" \
  "L_dna0_%=:\n" \
  "ds_read_b64 v[160:161], %[ab] offset:34304\n" \
  "ds_read_b128 v[152:155], %[ak] offset:32256\n" \
  "v_cvt_pk_bf16_f32 v128, v80, v81\n" \
  "v_cvt_pk_bf16_f32 v129, v82, v83\n" \
  "v_cvt_pk_bf16_f32 v130, v84, v85\n" \
  "v_cvt_pk_bf16_f32 v131, v86, v87\n" \
  "v_exp_f32 v88, v88\n" \
  "v_exp_f32 v89, v89\n" \
  "v_mfma_f32_32x32x16_bf16 %[ob], v[136:139], v[128:131], %[ob]\n" \
  "s_setprio 2\n" \
  "v_exp_f32 v90, v90\n" \
  "v_exp_f32 v91, v91\n" \
  "v_exp_f32 v92, v92\n" \
  "v_exp_f32 v93, v93\n" \
  "v_mfma_f32_32x32x16_bf16 v[112:127], v[156:159], %[qa1], v[112:127]\n" \
  "v_exp_f32 v94, v94\n" \
  "v_exp_f32 v95, v95\n" \
  "v_cvt_pk_bf16_f32 v132, v88, v89\n" \
  "v_cvt_pk_bf16_f32 v133, v90, v91\n" \
  "v_cvt_pk_bf16_f32 v134, v92, v93\n" \
  "v_cvt_pk_bf16_f32 v135, v94, v95\n" \
  "v_exp_f32 v96, v96\n" \
  "v_exp_f32 v97, v97\n" \
  "s_waitcnt lgkmcnt(0)\n" \
  "v_mfma_f32_32x32x16_bf16 %[ob], v[140:143], v[132:135], %[ob]\n" \
  "ds_read_b128 v[136:139], %[av] offset:34816\n" \
  "ds_read_b128 v[140:143], %[av] offset:35520\n" \
  "ds_read_b128 v[156:159], %[ak] offset:33280\n" \
  "v_exp_f32 v98, v98\n" \
  "v_exp_f32 v99, v99\n" \
  "v_exp_f32 v100, v100\n" \
  "v_mfma_f32_32x32x4_2b_bf16 v[64:95], v[160:161], %[qb], 0 cbsz:1 abid:0\n" \
  "v_exp_f32 v101, v101\n" \
  "v_exp_f32 v102, v102\n" \
  "v_exp_f32 v103, v103\n" \
  "v_cvt_pk_bf16_f32 v128, v96, v97\n" \
  "v_cvt_pk_bf16_f32 v129, v98, v99\n" \
  "v_cvt_pk_bf16_f32 v130, v100, v101\n" \
  "v_cvt_pk_bf16_f32 v131, v102, v103\n" \
  "v_exp_f32 v104, v104\n" \
  "v_exp_f32 v105, v105\n" \
  "v_exp_f32 v106, v106\n" \
  "v_mfma_f32_32x32x16_bf16 %[oa], v[144:147], v[128:131], %[oa]\n" \
  "v_exp_f32 v107, v107\n" \
  "v_exp_f32 v108, v108\n" \
  "v_exp_f32 v109, v109\n" \
  "v_exp_f32 v110, v110\n" \
  "v_exp_f32 v111, v111\n" \
  "v_add_u32 v163, 1, v163\n" \
  "s_waitcnt vmcnt(2)\n" \
  "ds_write_b32 %[ad], v163\n" \
  "v_cvt_pk_bf16_f32 v132, v104, v105\n" \
  "v_cvt_pk_bf16_f32 v133, v106, v107\n" \
  "v_cvt_pk_bf16_f32 v134, v108, v109\n" \
  "v_cvt_pk_bf16_f32 v135, v110, v111\n" \
  "v_mfma_f32_32x32x16_bf16 v[64:79], v[152:155], %[qa0], v[64:79]\n" \
  "v_exp_f32 v112, v112\n" \
  "v_exp_f32 v113, v113\n" \
  "v_exp_f32 v114, v114\n" \
  "v_exp_f32 v115, v115\n" \
  "v_mfma_f32_32x32x16_bf16 %[oa], v[148:151], v[132:135], %[oa]\n" \
  "s_setprio 0\n" \
  "v_exp_f32 v116, v116\n" \
  "v_exp_f32 v117, v117\n" \
  "v_exp_f32 v118, v118\n" \
  "v_exp_f32 v119, v119\n" \
  "v_add_u32 %[go], %[inc], %[go]\n" \
  "s_add_u32 %[nd], %[nd], 1\n" \
  "v_cvt_pk_bf16_f32 v128, v112, v113\n" \
  "v_cvt_pk_bf16_f32 v129, v114, v115\n" \
  "v_cvt_pk_bf16_f32 v130, v116, v117\n" \
  "v_cvt_pk_bf16_f32 v131, v118, v119\n" \
  "v_exp_f32 v120, v120\n" \
  "v_exp_f32 v121, v121\n" \
  "v_mfma_f32_32x32x16_bf16 %[ob], v[144:147], v[128:131], %[ob]\n" \
  "s_setprio 2\n" \
  "v_exp_f32 v122, v122\n" \
  "v_exp_f32 v123, v123\n" \
  "v_exp_f32 v124, v124\n" \
  "v_exp_f32 v125, v125\n" \
  "v_mfma_f32_32x32x16_bf16 v[80:95], v[152:155], %[qa1], v[80:95]\n" \
  "v_exp_f32 v126, v126\n" \
  "v_exp_f32 v127, v127\n" \
  "v_cvt_pk_bf16_f32 v132, v120, v121\n" \
  "v_cvt_pk_bf16_f32 v133, v122, v123\n" \
  "v_cvt_pk_bf16_f32 v134, v124, v125\n" \
  "v_cvt_pk_bf16_f32 v135, v126, v127\n" \
  "v_exp_f32 v64, v64\n" \
  "v_exp_f32 v65, v65\n" \
  "v_mfma_f32_32x32x16_bf16 %[ob], v[148:151], v[132:135], %[ob]\n" \
  "ds_read_b128 v[144:147], %[av] offset:36224\n" \
  "ds_read_b128 v[148:151], %[av] offset:36928\n" \
  "v_exp_f32 v66, v66\n" \
  "v_exp_f32 v67, v67\n" \
  "v_exp_f32 v68, v68\n" \
  "v_mfma_f32_32x32x4_2b_bf16 v[96:127], v[160:161], %[qb], 0 cbsz:1 abid:1\n" \
  "v_exp_f32 v69, v69\n" \
  "v_exp_f32 v70, v70\n" \
  "v_exp_f32 v71, v71\n" \
  "v_cvt_pk_bf16_f32 v128, v64, v65\n" \
  "v_cvt_pk_bf16_f32 v129, v66, v67\n" \
  "v_cvt_pk_bf16_f32 v130, v68, v69\n" \
  "v_cvt_pk_bf16_f32 v131, v70, v71\n" \
  "v_exp_f32 v72, v72\n" \
  "v_exp_f32 v73, v73\n" \
  "v_exp_f32 v74, v74\n" \
  "s_waitcnt lgkmcnt(2)\n" \
  "v_mfma_f32_32x32x16_bf16 %[oa], v[136:139], v[128:131], %[oa]\n" \
  "v_exp_f32 v75, v75\n" \
  "v_exp_f32 v76, v76\n" \
  "v_exp_f32 v77, v77\n" \
  "v_exp_f32 v78, v78\n" \
  "v_exp_f32 v79, v79\n" \
  "ds_read_b32 v162, %[af]\n" \
  "v_cvt_pk_bf16_f32 v132, v72, v73\n" \
  "v_cvt_pk_bf16_f32 v133, v74, v75\n" \
  "v_cvt_pk_bf16_f32 v134, v76, v77\n" \
  "v_cvt_pk_bf16_f32 v135, v78, v79\n" \
  "v_mfma_f32_32x32x16_bf16 v[96:111], v[156:159], %[qa0], v[96:111]\n" \
  "v_exp_f32 v80, v80\n" \
  "v_exp_f32 v81, v81\n" \
  "v_exp_f32 v82, v82\n" \
  "v_exp_f32 v83, v83\n" \
  "v_mfma_f32_32x32x16_bf16 %[oa], v[140:143], v[132:135], %[oa]\n" \
  "s_setprio 0\n" \
  "v_exp_f32 v84, v84\n" \
  "v_exp_f32 v85, v85\n" \
  "v_exp_f32 v86, v86\n" \
  "v_exp_f32 v87, v87\n" \
  "s_waitcnt lgkmcnt(0)\n" \
  "v_cmp_gt_u32 vcc, %[nd], v162\n" \
  "s_add_u32 m0, %[dst], 21504\n" \
  "s_cbranch_vccnz L_polla1_%=\n" \
  "L_oka1_%=:\n" \
  "s_cmp_eq_u32 %[cnt], 1\n" \
  "s_cbranch_scc1 L_noa1_%=\n" \
  "global_load_lds_dwordx4 %[go], %[src] sc1\n" \
  "global_load_lds_dwordx4 %[go], %[src] offset:320 sc1\n" \
  "L_dna1_%=:\n" \
  "ds_read_b64 v[160:161], %[ab] offset:2048\n" \
  "ds_read_b128 v[152:155], %[ak] offset:0\n" \
  "v_cvt_pk_bf16_f32 v128, v80, v81\n" \
  "v_cvt_pk_bf16_f32 v129, v82, v83\n" \
  "v_cvt_pk_bf16_f32 v130, v84, v85\n" \
  "v_cvt_pk_bf16_f32 v131, v86, v87\n" \
  "v_exp_f32 v88, v88\n" \
  "v_exp_f32 v89, v89\n" \
  "v_mfma_f32_32x32x16_bf16 %[ob], v[136:139], v[128:131], %[ob]\n" \
  "s_setprio 2\n" \
  "v_exp_f32 v90, v90\n" \
  "v_exp_f32 v91, v91\n" \
  "v_exp_f32 v92, v92\n" \
  "v_exp_f32 v93, v93\n" \
  "v_mfma_f32_32x32x16_bf16 v[112:127], v[156:159], %[qa1], v[112:127]\n" \
  "v_exp_f32 v94, v94\n" \
  "v_exp_f32 v95, v95\n" \
  "v_cvt_pk_bf16_f32 v132, v88, v89\n" \
  "v_cvt_pk_bf16_f32 v133, v90, v91\n" \
  "v_cvt_pk_bf16_f32 v134, v92, v93\n" \
  "v_cvt_pk_bf16_f32 v135, v94, v95\n" \
  "v_exp_f32 v96, v96\n" \
  "v_exp_f32 v97, v97\n" \
  "s_waitcnt lgkmcnt(0)\n" \
  "v_mfma_f32_32x32x16_bf16 %[ob], v[140:143], v[132:135], %[ob]\n" \
  "ds_read_b128 v[136:139], %[av] offset:2560\n" \
  "ds_read_b128 v[140:143], %[av] offset:3264\n" \
  "ds_read_b128 v[156:159], %[ak] offset:1024\n" \
  "v_exp_f32 v98, v98\n" \
  "v_exp_f32 v99, v99\n" \
  "v_exp_f32 v100, v100\n" \
  "v_mfma_f32_32x32x4_2b_bf16 v[64:95], v[160:161], %[qb], 0 cbsz:1 abid:0\n" \
  "v_exp_f32 v101, v101\n" \
  "v_exp_f32 v102, v102\n" \
  "v_exp_f32 v103, v103\n" \
  "v_cvt_pk_bf16_f32 v128, v96, v97\n" \
  "v_cvt_pk_bf16_f32 v129, v98, v99\n" \
  "v_cvt_pk_bf16_f32 v130, v100, v101\n" \
  "v_cvt_pk_bf16_f32 v131, v102, v103\n" \
  "v_exp_f32 v104, v104\n" \
  "v_exp_f32 v105, v105\n" \
  "v_exp_f32 v106, v106\n" \
  "v_mfma_f32_32x32x16_bf16 %[oa], v[144:147], v[128:131], %[oa]\n" \
  "v_exp_f32 v107, v107\n" \
  "v_exp_f32 v108, v108\n" \
  "v_exp_f32 v109, v109\n" \
  "v_exp_f32 v110, v110\n" \
  "v_exp_f32 v111, v111\n" \
  "v_add_u32 v163, 1, v163\n" \
  "s_waitcnt vmcnt(2)\n" \
  "ds_write_b32 %[ad], v163\n" \
  "v_cvt_pk_bf16_f32 v132, v104, v105\n" \
  "v_cvt_pk_bf16_f32 v133, v106, v107\n" \
  "v_cvt_pk_bf16_f32 v134, v108, v109\n" \
  "v_cvt_pk_bf16_f32 v135, v110, v111\n" \
  "v_mfma_f32_32x32x16_bf16 v[64:79], v[152:155], %[qa0], v[64:79]\n" \
  "v_exp_f32 v112, v112\n" \
  "v_exp_f32 v113, v113\n" \
  "v_exp_f32 v114, v114\n" \
  "v_exp_f32 v115, v115\n" \
  "v_mfma_f32_32x32x16_bf16 %[oa], v[148:151], v[132:135], %[oa]\n" \
  "s_setprio 0\n" \
  "v_exp_f32 v116, v116\n" \
  "v_exp_f32 v117, v117\n" \
  "v_exp_f32 v118, v118\n" \
  "v_exp_f32 v119, v119\n" \
  "v_add_u32 %[go], %[inc], %[go]\n" \
  "s_add_u32 %[nd], %[nd], 1\n" \
  "v_cvt_pk_bf16_f32 v128, v112, v113\n" \
  "v_cvt_pk_bf16_f32 v129, v114, v115\n" \
  "v_cvt_pk_bf16_f32 v130, v116, v117\n" \
  "v_cvt_pk_bf16_f32 v131, v118, v119\n" \
  "v_exp_f32 v120, v120\n" \
  "v_exp_f32 v121, v121\n" \
  "v_mfma_f32_32x32x16_bf16 %[ob], v[144:147], v[128:131], %[ob]\n" \
  "s_setprio 2\n" \
  "v_exp_f32 v122, v122\n" \
  "v_exp_f32 v123, v123\n" \
  "v_exp_f32 v124, v124\n" \
  "v_exp_f32 v125, v125\n" \
  "v_mfma_f32_32x32x16_bf16 v[80:95], v[152:155], %[qa1], v[80:95]\n" \
  "v_exp_f32 v126, v126\n" \
  "v_exp_f32 v127, v127\n" \
  "v_cvt_pk_bf16_f32 v132, v120, v121\n" \
  "v_cvt_pk_bf16_f32 v133, v122, v123\n" \
  "v_cvt_pk_bf16_f32 v134, v124, v125\n" \
  "v_cvt_pk_bf16_f32 v135, v126, v127\n" \
  "v_exp_f32 v64, v64\n" \
  "v_exp_f32 v65, v65\n" \
  "v_mfma_f32_32x32x16_bf16 %[ob], v[148:151], v[132:135], %[ob]\n" \
  "ds_read_b128 v[144:147], %[av] offset:3968\n" \
  "ds_read_b128 v[148:151], %[av] offset:4672\n" \
  "v_exp_f32 v66, v66\n" \
  "v_exp_f32 v67, v67\n" \
  "v_exp_f32 v68, v68\n" \
  "v_mfma_f32_32x32x4_2b_bf16 v[96:127], v[160:161], %[qb], 0 cbsz:1 abid:1\n" \
  "v_exp_f32 v69, v69\n" \
  "v_exp_f32 v70, v70\n" \
  "v_exp_f32 v71, v71\n" \
  "v_cvt_pk_bf16_f32 v128, v64, v65\n" \
  "v_cvt_pk_bf16_f32 v129, v66, v67\n" \
  "v_cvt_pk_bf16_f32 v130, v68, v69\n" \
  "v_cvt_pk_bf16_f32 v131, v70, v71\n" \
  "v_exp_f32 v72, v72\n" \
  "v_exp_f32 v73, v73\n" \
  "v_exp_f32 v74, v74\n" \
  "s_waitcnt lgkmcnt(2)\n" \
  "v_mfma_f32_32x32x16_bf16 %[oa], v[136:139], v[128:131], %[oa]\n" \
  "v_exp_f32 v75, v75\n" \
  "v_exp_f32 v76, v76\n" \
  "v_exp_f32 v77, v77\n" \
  "v_exp_f32 v78, v78\n" \
  "v_exp_f32 v79, v79\n" \
  "ds_read_b32 v162, %[af]\n" \
  "v_cvt_pk_bf16_f32 v132, v72, v73\n" \
  "v_cvt_pk_bf16_f32 v133, v74, v75\n" \
  "v_cvt_pk_bf16_f32 v134, v76, v77\n" \
  "v_cvt_pk_bf16_f32 v135, v78, v79\n" \
  "v_mfma_f32_32x32x16_bf16 v[96:111], v[156:159], %[qa0], v[96:111]\n" \
  "v_exp_f32 v80, v80\n" \
  "v_exp_f32 v81, v81\n" \
  "v_exp_f32 v82, v82\n" \
  "v_exp_f32 v83, v83\n" \
  "v_mfma_f32_32x32x16_bf16 %[oa], v[140:143], v[132:135], %[oa]\n" \
  "s_setprio 0\n" \
  "v_exp_f32 v84, v84\n" \
  "v_exp_f32 v85, v85\n" \
  "v_exp_f32 v86, v86\n" \
  "v_exp_f32 v87, v87\n" \
  "s_waitcnt lgkmcnt(0)\n" \
  "v_cmp_gt_u32 vcc, %[nd], v162\n" \
  "s_add_u32 m0, %[dst], 32256\n" \
  "s_cbranch_vccnz L_polla2_%=\n" \
  "L_oka2_%=:\n" \
  "s_cmp_eq_u32 %[cnt], 1\n" \
  "s_cbranch_scc1 L_noa2_%=\n" \
  "global_load_lds_dwordx4 %[go], %[src] sc1\n" \
  "global_load_lds_dwordx4 %[go], %[src] offset:320 sc1\n" \
  "L_dna2_%=:\n" \
  "ds_read_b64 v[160:161], %[ab] offset:12800\n" \
  "ds_read_b128 v[152:155], %[ak] offset:10752\n" \
  "v_cvt_pk_bf16_f32 v128, v80, v81\n" \
  "v_cvt_pk_bf16_f32 v129, v82, v83\n" \
  "v_cvt_pk_bf16_f32 v130, v84, v85\n" \
  "v_cvt_pk_bf16_f32 v131, v86, v87\n" \
  "v_exp_f32 v88, v88\n" \
  "v_exp_f32 v89, v89\n" \
  "v_mfma_f32_32x32x16_bf16 %[ob], v[136:139], v[128:131], %[ob]\n" \
  "s_setprio 2\n" \
  "v_exp_f32 v90, v90\n" \
  "v_exp_f32 v91, v91\n" \
  "v_exp_f32 v92, v92\n" \
  "v_exp_f32 v93, v93\n" \
  "v_mfma_f32_32x32x16_bf16 v[112:127], v[156:159], %[qa1], v[112:127]\n" \
  "v_exp_f32 v94, v94\n" \
  "v_exp_f32 v95, v95\n" \
  "v_cvt_pk_bf16_f32 v132, v88, v89\n" \
  "v_cvt_pk_bf16_f32 v133, v90, v91\n" \
  "v_cvt_pk_bf16_f32 v134, v92, v93\n" \
  "v_cvt_pk_bf16_f32 v135, v94, v95\n" \
  "v_exp_f32 v96, v96\n" \
  "v_exp_f32 v97, v97\n" \
  "s_waitcnt lgkmcnt(0)\n" \
  "v_mfma_f32_32x32x16_bf16 %[ob], v[140:143], v[132:135], %[ob]\n" \
  "ds_read_b128 v[136:139], %[av] offset:13312\n" \
  "ds_read_b128 v[140:143], %[av] offset:14016\n" \
  "ds_read_b128 v[156:159], %[ak] offset:11776\n" \
  "v_exp_f32 v98, v98\n" \
  "v_exp_f32 v99, v99\n" \
  "v_exp_f32 v100, v100\n" \
  "v_mfma_f32_32x32x4_2b_bf16 v[64:95], v[160:161], %[qb], 0 cbsz:1 abid:0\n" \
  "v_exp_f32 v101, v101\n" \
  "v_exp_f32 v102, v102\n" \
  "v_exp_f32 v103, v103\n" \
  "v_cvt_pk_bf16_f32 v128, v96, v97\n" \
  "v_cvt_pk_bf16_f32 v129, v98, v99\n" \
  "v_cvt_pk_bf16_f32 v130, v100, v101\n" \
  "v_cvt_pk_bf16_f32 v131, v102, v103\n" \
  "v_exp_f32 v104, v104\n" \
  "v_exp_f32 v105, v105\n" \
  "v_exp_f32 v106, v106\n" \
  "v_mfma_f32_32x32x16_bf16 %[oa], v[144:147], v[128:131], %[oa]\n" \
  "v_exp_f32 v107, v107\n" \
  "v_exp_f32 v108, v108\n" \
  "v_exp_f32 v109, v109\n" \
  "v_exp_f32 v110, v110\n" \
  "v_exp_f32 v111, v111\n" \
  "v_add_u32 v163, 1, v163\n" \
  "s_waitcnt vmcnt(2)\n" \
  "ds_write_b32 %[ad], v163\n" \
  "v_cvt_pk_bf16_f32 v132, v104, v105\n" \
  "v_cvt_pk_bf16_f32 v133, v106, v107\n" \
  "v_cvt_pk_bf16_f32 v134, v108, v109\n" \
  "v_cvt_pk_bf16_f32 v135, v110, v111\n" \
  "v_mfma_f32_32x32x16_bf16 v[64:79], v[152:155], %[qa0], v[64:79]\n" \
  "v_exp_f32 v112, v112\n" \
  "v_exp_f32 v113, v113\n" \
  "v_exp_f32 v114, v114\n" \
  "v_exp_f32 v115, v115\n" \
  "v_mfma_f32_32x32x16_bf16 %[oa], v[148:151], v[132:135], %[oa]\n" \
  "s_setprio 0\n" \
  "v_exp_f32 v116, v116\n" \
  "v_exp_f32 v117, v117\n" \
  "v_exp_f32 v118, v118\n" \
  "v_exp_f32 v119, v119\n" \
  "v_add_u32 %[go], %[inc], %[go]\n" \
  "s_add_u32 %[nd], %[nd], 1\n" \
  "v_cvt_pk_bf16_f32 v128, v112, v113\n" \
  "v_cvt_pk_bf16_f32 v129, v114, v115\n" \
  "v_cvt_pk_bf16_f32 v130, v116, v117\n" \
  "v_cvt_pk_bf16_f32 v131, v118, v119\n" \
  "v_exp_f32 v120, v120\n" \
  "v_exp_f32 v121, v121\n" \
  "v_mfma_f32_32x32x16_bf16 %[ob], v[144:147], v[128:131], %[ob]\n" \
  "s_setprio 2\n" \
  "v_exp_f32 v122, v122\n" \
  "v_exp_f32 v123, v123\n" \
  "v_exp_f32 v124, v124\n" \
  "v_exp_f32 v125, v125\n" \
  "v_mfma_f32_32x32x16_bf16 v[80:95], v[152:155], %[qa1], v[80:95]\n" \
  "v_exp_f32 v126, v126\n" \
  "v_exp_f32 v127, v127\n" \
  "v_cvt_pk_bf16_f32 v132, v120, v121\n" \
  "v_cvt_pk_bf16_f32 v133, v122, v123\n" \
  "v_cvt_pk_bf16_f32 v134, v124, v125\n" \
  "v_cvt_pk_bf16_f32 v135, v126, v127\n" \
  "v_exp_f32 v64, v64\n" \
  "v_exp_f32 v65, v65\n" \
  "v_mfma_f32_32x32x16_bf16 %[ob], v[148:151], v[132:135], %[ob]\n" \
  "ds_read_b128 v[144:147], %[av] offset:14720\n" \
  "ds_read_b128 v[148:151], %[av] offset:15424\n" \
  "v_exp_f32 v66, v66\n" \
  "v_exp_f32 v67, v67\n" \
  "v_exp_f32 v68, v68\n" \
  "v_mfma_f32_32x32x4_2b_bf16 v[96:127], v[160:161], %[qb], 0 cbsz:1 abid:1\n" \
  "v_exp_f32 v69, v69\n" \
  "v_exp_f32 v70, v70\n" \
  "v_exp_f32 v71, v71\n" \
  "v_cvt_pk_bf16_f32 v128, v64, v65\n" \
  "v_cvt_pk_bf16_f32 v129, v66, v67\n" \
  "v_cvt_pk_bf16_f32 v130, v68, v69\n" \
  "v_cvt_pk_bf16_f32 v131, v70, v71\n" \
  "v_exp_f32 v72, v72\n" \
  "v_exp_f32 v73, v73\n" \
  "v_exp_f32 v74, v74\n" \
  "s_waitcnt lgkmcnt(2)\n" \
  "v_mfma_f32_32x32x16_bf16 %[oa], v[136:139], v[128:131], %[oa]\n" \
  "v_exp_f32 v75, v75\n" \
  "v_exp_f32 v76, v76\n" \
  "v_exp_f32 v77, v77\n" \
  "v_exp_f32 v78, v78\n" \
  "v_exp_f32 v79, v79\n" \
  "ds_read_b32 v162, %[af]\n" \
  "v_cvt_pk_bf16_f32 v132, v72, v73\n" \
  "v_cvt_pk_bf16_f32 v133, v74, v75\n" \
  "v_cvt_pk_bf16_f32 v134, v76, v77\n" \
  "v_cvt_pk_bf16_f32 v135, v78, v79\n" \
  "v_mfma_f32_32x32x16_bf16 v[96:111], v[156:159], %[qa0], v[96:111]\n" \
  "v_exp_f32 v80, v80\n" \
  "v_exp_f32 v81, v81\n" \
  "v_exp_f32 v82, v82\n" \
  "v_exp_f32 v83, v83\n" \
  "v_mfma_f32_32x32x16_bf16 %[oa], v[140:143], v[132:135], %[oa]\n" \
  "s_setprio 0\n" \
  "v_exp_f32 v84, v84\n" \
  "v_exp_f32 v85, v85\n" \
  "v_exp_f32 v86, v86\n" \
  "v_exp_f32 v87, v87\n" \
  "s_waitcnt lgkmcnt(0)\n" \
  "v_cmp_gt_u32 vcc, %[nd], v162\n" \
  "s_add_u32 m0, %[dst], 0\n" \
  "s_cbranch_vccnz L_polla3_%=\n" \
  "L_oka3_%=:\n" \
  "s_cmp_eq_u32 %[cnt], 1\n" \
  "s_cbranch_scc1 L_noa3_%=\n" \
  "global_load_lds_dwordx4 %[go], %[src] sc1\n" \
  "global_load_lds_dwordx4 %[go], %[src] offset:320 sc1\n" \
  "L_dna3_%=:\n" \
  "ds_read_b64 v[160:161], %[ab] offset:23552\n" \
  "ds_read_b128 v[152:155], %[ak] offset:21504\n" \
  "v_cvt_pk_bf16_f32 v128, v80, v81\n" \
  "v_cvt_pk_bf16_f32 v129, v82, v83\n" \
  "v_cvt_pk_bf16_f32 v130, v84, v85\n" \
  "v_cvt_pk_bf16_f32 v131, v86, v87\n" \
  "v_exp_f32 v88, v88\n" \
  "v_exp_f32 v89, v89\n" \
  "v_mfma_f32_32x32x16_bf16 %[ob], v[136:139], v[128:131], %[ob]\n" \
  "s_setprio 2\n" \
  "v_exp_f32 v90, v90\n" \
  "v_exp_f32 v91, v91\n" \
  "v_exp_f32 v92, v92\n" \
  "v_exp_f32 v93, v93\n" \
  "v_mfma_f32_32x32x16_bf16 v[112:127], v[156:159], %[qa1], v[112:127]\n" \
  "v_exp_f32 v94, v94\n" \
  "v_exp_f32 v95, v95\n" \
  "v_cvt_pk_bf16_f32 v132, v88, v89\n" \
  "v_cvt_pk_bf16_f32 v133, v90, v91\n" \
  "v_cvt_pk_bf16_f32 v134, v92, v93\n" \
  "v_cvt_pk_bf16_f32 v135, v94, v95\n" \
  "v_exp_f32 v96, v96\n" \
  "v_exp_f32 v97, v97\n" \
  "s_waitcnt lgkmcnt(0)\n" \
  "v_mfma_f32_32x32x16_bf16 %[ob], v[140:143], v[132:135], %[ob]\n" \
  "ds_read_b128 v[136:139], %[av] offset:24064\n" \
  "ds_read_b128 v[140:143], %[av] offset:24768\n" \
  "ds_read_b128 v[156:159], %[ak] offset:22528\n" \
  "v_exp_f32 v98, v98\n" \
  "v_exp_f32 v99, v99\n" \
  "v_exp_f32 v100, v100\n" \
  "v_mfma_f32_32x32x4_2b_bf16 v[64:95], v[160:161], %[qb], 0 cbsz:1 abid:0\n" \
  "v_exp_f32 v101, v101\n" \
  "v_exp_f32 v102, v102\n" \
  "v_exp_f32 v103, v103\n" \
  "v_cvt_pk_bf16_f32 v128, v96, v97\n" \
  "v_cvt_pk_bf16_f32 v129, v98, v99\n" \
  "v_cvt_pk_bf16_f32 v130, v100, v101\n" \
  "v_cvt_pk_bf16_f32 v131, v102, v103\n" \
  "v_exp_f32 v104, v104\n" \
  "v_exp_f32 v105, v105\n" \
  "v_exp_f32 v106, v106\n" \
  "v_mfma_f32_32x32x16_bf16 %[oa], v[144:147], v[128:131], %[oa]\n" \
  "v_exp_f32 v107, v107\n" \
  "v_exp_f32 v108, v108\n" \
  "v_exp_f32 v109, v109\n" \
  "v_exp_f32 v110, v110\n" \
  "v_exp_f32 v111, v111\n" \
  "v_add_u32 v163, 1, v163\n" \
  "s_waitcnt vmcnt(2)\n" \
  "ds_write_b32 %[ad], v163\n" \
  "v_cvt_pk_bf16_f32 v132, v104, v105\n" \
  "v_cvt_pk_bf16_f32 v133, v106, v107\n" \
  "v_cvt_pk_bf16_f32 v134, v108, v109\n" \
  "v_cvt_pk_bf16_f32 v135, v110, v111\n" \
  "v_mfma_f32_32x32x16_bf16 v[64:79], v[152:155], %[qa0], v[64:79]\n" \
  "v_exp_f32 v112, v112\n" \
  "v_exp_f32 v113, v113\n" \
  "v_exp_f32 v114, v114\n" \
  "v_exp_f32 v115, v115\n" \
  "v_mfma_f32_32x32x16_bf16 %[oa], v[148:151], v[132:135], %[oa]\n" \
  "s_setprio 0\n" \
  "v_exp_f32 v116, v116\n" \
  "v_exp_f32 v117, v117\n" \
  "v_exp_f32 v118, v118\n" \
  "v_exp_f32 v119, v119\n" \
  "v_add_u32 %[go], %[inc], %[go]\n" \
  "s_add_u32 %[nd], %[nd], 1\n" \
  "v_cvt_pk_bf16_f32 v128, v112, v113\n" \
  "v_cvt_pk_bf16_f32 v129, v114, v115\n" \
  "v_cvt_pk_bf16_f32 v130, v116, v117\n" \
  "v_cvt_pk_bf16_f32 v131, v118, v119\n" \
  "v_exp_f32 v120, v120\n" \
  "v_exp_f32 v121, v121\n" \
  "v_mfma_f32_32x32x16_bf16 %[ob], v[144:147], v[128:131], %[ob]\n" \
  "s_setprio 2\n" \
  "v_exp_f32 v122, v122\n" \
  "v_exp_f32 v123, v123\n" \
  "v_exp_f32 v124, v124\n" \
  "v_exp_f32 v125, v125\n" \
  "v_mfma_f32_32x32x16_bf16 v[80:95], v[152:155], %[qa1], v[80:95]\n" \
  "v_exp_f32 v126, v126\n" \
  "v_exp_f32 v127, v127\n" \
  "v_cvt_pk_bf16_f32 v132, v120, v121\n" \
  "v_cvt_pk_bf16_f32 v133, v122, v123\n" \
  "v_cvt_pk_bf16_f32 v134, v124, v125\n" \
  "v_cvt_pk_bf16_f32 v135, v126, v127\n" \
  "s_sub_u32 %[cnt], %[cnt], 1\n" \
  "s_cmp_lg_u32 %[cnt], 0\n" \
  "s_cbranch_scc1 L_loopa%=\n" \
  "s_nop 1\n" \
  "v_mfma_f32_32x32x16_bf16 %[ob], v[148:151], v[132:135], %[ob]\n" \
  "s_waitcnt lgkmcnt(0)\n" \
  "s_branch L_end%=\n" \
  "L_polla0_%=:\n" \
  "s_sub_u32 %[cap], %[cap], 1\n" \
  "s_cbranch_scc1 L_oka0_%=\n" \
  "s_sleep 1\n" \
  "ds_read_b32 v162, %[af]\n" \
  "s_waitcnt lgkmcnt(0)\n" \
  "v_cmp_gt_u32 vcc, %[nd], v162\n" \
  "s_cbranch_vccnz L_polla0_%=\n" \
  "s_branch L_oka0_%=\n" \
  "L_noa1_%=:\n" \
  "s_waitcnt vmcnt(0)\n" \
  "s_branch L_dna1_%=\n" \
  "L_polla1_%=:\n" \
  "s_sub_u32 %[cap], %[cap], 1\n" \
  "s_cbranch_scc1 L_oka1_%=\n" \
  "s_sleep 1\n" \
  "ds_read_b32 v162, %[af]\n" \
  "s_waitcnt lgkmcnt(0)\n" \
  "v_cmp_gt_u32 vcc, %[nd], v162\n" \
  "s_cbranch_vccnz L_polla1_%=\n" \
  "s_branch L_oka1_%=\n" \
  "L_noa2_%=:\n" \
  "s_waitcnt vmcnt(0)\n" \
  "s_branch L_dna2_%=\n" \
  "L_polla2_%=:\n" \
  "s_sub_u32 %[cap], %[cap], 1\n" \
  "s_cbranch_scc1 L_oka2_%=\n" \
  "s_sleep 1\n" \
  "ds_read_b32 v162, %[af]\n" \
  "s_waitcnt lgkmcnt(0)\n" \
  "v_cmp_gt_u32 vcc, %[nd], v162\n" \
  "s_cbranch_vccnz L_polla2_%=\n" \
  "s_branch L_oka2_%=\n" \
  "L_noa3_%=:\n" \
  "s_waitcnt vmcnt(0)\n" \
  "s_branch L_dna3_%=\n" \
  "L_polla3_%=:\n" \
  "s_sub_u32 %[cap], %[cap], 1\n" \
  "s_cbranch_scc1 L_oka3_%=\n" \
  "s_sleep 1\n" \
  "ds_read_b32 v162, %[af]\n" \
  "s_waitcnt lgkmcnt(0)\n" \
  "v_cmp_gt_u32 vcc, %[nd], v162\n" \
  "s_cbranch_vccnz L_polla3_%=\n" \
  "s_branch L_oka3_%=\n" \
  "L_b%=:\n" \
  "s_setprio 1\n" \
  "v_mov_b32 v132, 0\n" \
  "v_mov_b32 v133, 0\n" \
  "v_mov_b32 v134, 0\n" \
  "v_mov_b32 v135, 0\n" \
  "v_mov_b32 v163, 1\n" \
  "ds_write_b32 %[ad], v163\n" \
  "s_mov_b32 m0, %[dst]\n" \
  "s_nop 0\n" \
  "global_load_lds_dwordx4 %[go], %[src] sc1\n" \
  "global_load_lds_dwordx4 %[go], %[src] offset:320 sc1\n" \
  "v_add_u32 %[go], 10752, %[go]\n" \
  "ds_read_b64 v[160:161], %[ab] offset:23552\n" \
  "ds_read_b128 v[152:155], %[ak] offset:21504\n" \
  "ds_read_b128 v[148:151], %[av] offset:24064\n" \
  "ds_read_b128 v[136:139], %[av] offset:24064\n" \
  "ds_read_b128 v[140:143], %[av] offset:24768\n" \
  "ds_read_b128 v[156:159], %[ak] offset:22528\n" \
  "s_waitcnt lgkmcnt(5)\n" \
  "v_mfma_f32_32x32x4_2b_bf16 v[64:95], v[160:161], %[qb], 0 cbsz:1 abid:0\n" \
  "s_nop 15\n" \
  "s_nop 2\n" \
  "s_waitcnt lgkmcnt(4)\n" \
  "v_mfma_f32_32x32x16_bf16 v[64:79], v[152:155], %[qa0], v[64:79]\n" \
  "v_mfma_f32_32x32x16_bf16 v[80:95], v[152:155], %[qa1], v[80:95]\n" \
  "s_waitcnt lgkmcnt(0)\n" \
  "s_nop 7\n" \
  "s_mov_b32 %[cnt], 4\n" \
  "s_mov_b32 %[nd], 1\n" \
  "s_movk_i32 %[inc], 10752\n" \
  "L_loopb%=:\n" \
  "v_exp_f32 v64, v64\n" \
  "v_exp_f32 v65, v65\n" \
  "v_mfma_f32_32x32x16_bf16 %[ob], v[148:151], v[132:135], %[ob]\n" \
  "ds_read_b128 v[144:147], %[av] offset:25472\n" \
  "ds_read_b128 v[148:151], %[av] offset:26176\n" \
  "v_exp_f32 v66, v66\n" \
  "v_exp_f32 v67, v67\n" \
  "v_exp_f32 v68, v68\n" \
  "v_mfma_f32_32x32x4_2b_bf16 v[96:127], v[160:161], %[qb], 0 cbsz:1 abid:1\n" \
  "v_exp_f32 v69, v69\n" \
  "v_exp_f32 v70, v70\n" \
  "v_exp_f32 v71, v71\n" \
  "v_cvt_pk_bf16_f32 v128, v64, v65\n" \
  "v_cvt_pk_bf16_f32 v129, v66, v67\n" \
  "v_cvt_pk_bf16_f32 v130, v68, v69\n" \
  "v_cvt_pk_bf16_f32 v131, v70, v71\n" \
  "v_exp_f32 v72, v72\n" \
  "v_exp_f32 v73, v73\n" \
  "v_exp_f32 v74, v74\n" \
  "s_waitcnt lgkmcnt(2)\n" \
  "v_mfma_f32_32x32x16_bf16 %[oa], v[136:139], v[128:131], %[oa]\n" \
  "v_exp_f32 v75, v75\n" \
  "v_exp_f32 v76, v76\n" \
  "v_exp_f32 v77, v77\n" \
  "v_exp_f32 v78, v78\n" \
  "v_exp_f32 v79, v79\n" \
  "ds_read_b32 v162, %[af]\n" \
  "v_cvt_pk_bf16_f32 v132, v72, v73\n" \
  "v_cvt_pk_bf16_f32 v133, v74, v75\n" \
  "v_cvt_pk_bf16_f32 v134, v76, v77\n" \
  "v_cvt_pk_bf16_f32 v135, v78, v79\n" \
  "v_mfma_f32_32x32x16_bf16 v[96:111], v[156:159], %[qa0], v[96:111]\n" \
  "v_exp_f32 v80, v80\n" \
  "v_exp_f32 v81, v81\n" \
  "v_exp_f32 v82, v82\n" \
  "v_exp_f32 v83, v83\n" \
  "v_mfma_f32_32x32x16_bf16 %[oa], v[140:143], v[132:135], %[oa]\n" \
  "v_exp_f32 v84, v84\n" \
  "v_exp_f32 v85, v85\n" \
  "v_exp_f32 v86, v86\n" \
  "v_exp_f32 v87, v87\n" \
  "s_waitcnt lgkmcnt(0)\n" \
  "v_cmp_gt_u32 vcc, %[nd], v162\n" \
  "s_add_u32 m0, %[dst], 10752\n" \
  "s_cbranch_vccnz L_pollb0_%=\n" \
  "L_okb0_%=:\n" \
  "global_load_lds_dwordx4 %[go], %[src] sc1\n" \
  "global_load_lds_dwordx4 %[go], %[src] offset:320 sc1\n" \
  "L_dnb0_%=:\n" \
  "ds_read_b64 v[160:161], %[ab] offset:34304\n" \
  "ds_read_b128 v[152:155], %[ak] offset:32256\n" \
  "v_cvt_pk_bf16_f32 v128, v80, v81\n" \
  "v_cvt_pk_bf16_f32 v129, v82, v83\n" \
  "v_cvt_pk_bf16_f32 v130, v84, v85\n" \
  "v_cvt_pk_bf16_f32 v131, v86, v87\n" \
  "v_exp_f32 v88, v88\n" \
  "v_exp_f32 v89, v89\n" \
  "v_mfma_f32_32x32x16_bf16 %[ob], v[136:139], v[128:131], %[ob]\n" \
  "v_exp_f32 v90, v90\n" \
  "v_exp_f32 v91, v91\n" \
  "v_exp_f32 v92, v92\n" \
  "v_exp_f32 v93, v93\n" \
  "v_mfma_f32_32x32x16_bf16 v[112:127], v[156:159], %[qa1], v[112:127]\n" \
  "v_exp_f32 v94, v94\n" \
  "v_exp_f32 v95, v95\n" \
  "v_cvt_pk_bf16_f32 v132, v88, v89\n" \
  "v_cvt_pk_bf16_f32 v133, v90, v91\n" \
  "v_cvt_pk_bf16_f32 v134, v92, v93\n" \
  "v_cvt_pk_bf16_f32 v135, v94, v95\n" \
  "v_exp_f32 v96, v96\n" \
  "v_exp_f32 v97, v97\n" \
  "s_waitcnt lgkmcnt(0)\n" \
  "v_mfma_f32_32x32x16_bf16 %[ob], v[140:143], v[132:135], %[ob]\n" \
  "ds_read_b128 v[136:139], %[av] offset:34816\n" \
  "ds_read_b128 v[140:143], %[av] offset:35520\n" \
  "ds_read_b128 v[156:159], %[ak] offset:33280\n" \
  "v_exp_f32 v98, v98\n" \
  "v_exp_f32 v99, v99\n" \
  "v_exp_f32 v100, v100\n" \
  "v_mfma_f32_32x32x4_2b_bf16 v[64:95], v[160:161], %[qb], 0 cbsz:1 abid:0\n" \
  "v_exp_f32 v101, v101\n" \
  "v_exp_f32 v102, v102\n" \
  "v_exp_f32 v103, v103\n" \
  "v_cvt_pk_bf16_f32 v128, v96, v97\n" \
  "v_cvt_pk_bf16_f32 v129, v98, v99\n" \
  "v_cvt_pk_bf16_f32 v130, v100, v101\n" \
  "v_cvt_pk_bf16_f32 v131, v102, v103\n" \
  "v_exp_f32 v104, v104\n" \
  "v_exp_f32 v105, v105\n" \
  "v_exp_f32 v106, v106\n" \
  "v_mfma_f32_32x32x16_bf16 %[oa], v[144:147], v[128:131], %[oa]\n" \
  "v_exp_f32 v107, v107\n" \
  "v_exp_f32 v108, v108\n" \
  "v_exp_f32 v109, v109\n" \
  "v_exp_f32 v110, v110\n" \
  "v_exp_f32 v111, v111\n" \
  "v_add_u32 v163, 1, v163\n" \
  "s_waitcnt vmcnt(2)\n" \
  "ds_write_b32 %[ad], v163\n" \
  "v_cvt_pk_bf16_f32 v132, v104, v105\n" \
  "v_cvt_pk_bf16_f32 v133, v106, v107\n" \
  "v_cvt_pk_bf16_f32 v134, v108, v109\n" \
  "v_cvt_pk_bf16_f32 v135, v110, v111\n" \
  "v_mfma_f32_32x32x16_bf16 v[64:79], v[152:155], %[qa0], v[64:79]\n" \
  "v_exp_f32 v112, v112\n" \
  "v_exp_f32 v113, v113\n" \
  "v_exp_f32 v114, v114\n" \
  "v_exp_f32 v115, v115\n" \
  "v_mfma_f32_32x32x16_bf16 %[oa], v[148:151], v[132:135], %[oa]\n" \
  "v_exp_f32 v116, v116\n" \
  "v_exp_f32 v117, v117\n" \
  "v_exp_f32 v118, v118\n" \
  "v_exp_f32 v119, v119\n" \
  "v_add_u32 %[go], %[inc], %[go]\n" \
  "s_add_u32 %[nd], %[nd], 1\n" \
  "v_cvt_pk_bf16_f32 v128, v112, v113\n" \
  "v_cvt_pk_bf16_f32 v129, v114, v115\n" \
  "v_cvt_pk_bf16_f32 v130, v116, v117\n" \
  "v_cvt_pk_bf16_f32 v131, v118, v119\n" \
  "v_exp_f32 v120, v120\n" \
  "v_exp_f32 v121, v121\n" \
  "v_mfma_f32_32x32x16_bf16 %[ob], v[144:147], v[128:131], %[ob]\n" \
  "v_exp_f32 v122, v122\n" \
  "v_exp_f32 v123, v123\n" \
  "v_exp_f32 v124, v124\n" \
  "v_exp_f32 v125, v125\n" \
  "v_mfma_f32_32x32x16_bf16 v[80:95], v[152:155], %[qa1], v[80:95]\n" \
  "v_exp_f32 v126, v126\n" \
  "v_exp_f32 v127, v127\n" \
  "v_cvt_pk_bf16_f32 v132, v120, v121\n" \
  "v_cvt_pk_bf16_f32 v133, v122, v123\n" \
  "v_cvt_pk_bf16_f32 v134, v124, v125\n" \
  "v_cvt_pk_bf16_f32 v135, v126, v127\n" \
  "v_exp_f32 v64, v64\n" \
  "v_exp_f32 v65, v65\n" \
  "v_mfma_f32_32x32x16_bf16 %[ob], v[148:151], v[132:135], %[ob]\n" \
  "ds_read_b128 v[144:147], %[av] offset:36224\n" \
  "ds_read_b128 v[148:151], %[av] offset:36928\n" \
  "v_exp_f32 v66, v66\n" \
  "v_exp_f32 v67, v67\n" \
  "v_exp_f32 v68, v68\n" \
  "v_mfma_f32_32x32x4_2b_bf16 v[96:127], v[160:161], %[qb], 0 cbsz:1 abid:1\n" \
  "v_exp_f32 v69, v69\n" \
  "v_exp_f32 v70, v70\n" \
  "v_exp_f32 v71, v71\n" \
  "v_cvt_pk_bf16_f32 v128, v64, v65\n" \
  "v_cvt_pk_bf16_f32 v129, v66, v67\n" \
  "v_cvt_pk_bf16_f32 v130, v68, v69\n" \
  "v_cvt_pk_bf16_f32 v131, v70, v71\n" \
  "v_exp_f32 v72, v72\n" \
  "v_exp_f32 v73, v73\n" \
  "v_exp_f32 v74, v74\n" \
  "s_waitcnt lgkmcnt(2)\n" \
  "v_mfma_f32_32x32x16_bf16 %[oa], v[136:139], v[128:131], %[oa]\n" \
  "v_exp_f32 v75, v75\n" \
  "v_exp_f32 v76, v76\n" \
  "v_exp_f32 v77, v77\n" \
  "v_exp_f32 v78, v78\n" \
  "v_exp_f32 v79, v79\n" \
  "ds_read_b32 v162, %[af]\n" \
  "v_cvt_pk_bf16_f32 v132, v72, v73\n" \
  "v_cvt_pk_bf16_f32 v133, v74, v75\n" \
  "v_cvt_pk_bf16_f32 v134, v76, v77\n" \
  "v_cvt_pk_bf16_f32 v135, v78, v79\n" \
  "v_mfma_f32_32x32x16_bf16 v[96:111], v[156:159], %[qa0], v[96:111]\n" \
  "v_exp_f32 v80, v80\n" \
  "v_exp_f32 v81, v81\n" \
  "v_exp_f32 v82, v82\n" \
  "v_exp_f32 v83, v83\n" \
  "v_mfma_f32_32x32x16_bf16 %[oa], v[140:143], v[132:135], %[oa]\n" \
  "v_exp_f32 v84, v84\n" \
  "v_exp_f32 v85, v85\n" \
  "v_exp_f32 v86, v86\n" \
  "v_exp_f32 v87, v87\n" \
  "s_waitcnt lgkmcnt(0)\n" \
  "v_cmp_gt_u32 vcc, %[nd], v162\n" \
  "s_add_u32 m0, %[dst], 21504\n" \
  "s_cbranch_vccnz L_pollb1_%=\n" \
  "L_okb1_%=:\n" \
  "s_cmp_eq_u32 %[cnt], 1\n" \
  "s_cbranch_scc1 L_nob1_%=\n" \
  "global_load_lds_dwordx4 %[go], %[src] sc1\n" \
  "global_load_lds_dwordx4 %[go], %[src] offset:320 sc1\n" \
  "L_dnb1_%=:\n" \
  "ds_read_b64 v[160:161], %[ab] offset:2048\n" \
  "ds_read_b128 v[152:155], %[ak] offset:0\n" \
  "v_cvt_pk_bf16_f32 v128, v80, v81\n" \
  "v_cvt_pk_bf16_f32 v129, v82, v83\n" \
  "v_cvt_pk_bf16_f32 v130, v84, v85\n" \
  "v_cvt_pk_bf16_f32 v131, v86, v87\n" \
  "v_exp_f32 v88, v88\n" \
  "v_exp_f32 v89, v89\n" \
  "v_mfma_f32_32x32x16_bf16 %[ob], v[136:139], v[128:131], %[ob]\n" \
  "v_exp_f32 v90, v90\n" \
  "v_exp_f32 v91, v91\n" \
  "v_exp_f32 v92, v92\n" \
  "v_exp_f32 v93, v93\n" \
  "v_mfma_f32_32x32x16_bf16 v[112:127], v[156:159], %[qa1], v[112:127]\n" \
  "v_exp_f32 v94, v94\n" \
  "v_exp_f32 v95, v95\n" \
  "v_cvt_pk_bf16_f32 v132, v88, v89\n" \
  "v_cvt_pk_bf16_f32 v133, v90, v91\n" \
  "v_cvt_pk_bf16_f32 v134, v92, v93\n" \
  "v_cvt_pk_bf16_f32 v135, v94, v95\n" \
  "v_exp_f32 v96, v96\n" \
  "v_exp_f32 v97, v97\n" \
  "s_waitcnt lgkmcnt(0)\n" \
  "v_mfma_f32_32x32x16_bf16 %[ob], v[140:143], v[132:135], %[ob]\n" \
  "ds_read_b128 v[136:139], %[av] offset:2560\n" \
  "ds_read_b128 v[140:143], %[av] offset:3264\n" \
  "ds_read_b128 v[156:159], %[ak] offset:1024\n" \
  "v_exp_f32 v98, v98\n" \
  "v_exp_f32 v99, v99\n" \
  "v_exp_f32 v100, v100\n" \
  "v_mfma_f32_32x32x4_2b_bf16 v[64:95], v[160:161], %[qb], 0 cbsz:1 abid:0\n" \
  "v_exp_f32 v101, v101\n" \
  "v_exp_f32 v102, v102\n" \
  "v_exp_f32 v103, v103\n" \
  "v_cvt_pk_bf16_f32 v128, v96, v97\n" \
  "v_cvt_pk_bf16_f32 v129, v98, v99\n" \
  "v_cvt_pk_bf16_f32 v130, v100, v101\n" \
  "v_cvt_pk_bf16_f32 v131, v102, v103\n" \
  "v_exp_f32 v104, v104\n" \
  "v_exp_f32 v105, v105\n" \
  "v_exp_f32 v106, v106\n" \
  "v_mfma_f32_32x32x16_bf16 %[oa], v[144:147], v[128:131], %[oa]\n" \
  "v_exp_f32 v107, v107\n" \
  "v_exp_f32 v108, v108\n" \
  "v_exp_f32 v109, v109\n" \
  "v_exp_f32 v110, v110\n" \
  "v_exp_f32 v111, v111\n" \
  "v_add_u32 v163, 1, v163\n" \
  "s_waitcnt vmcnt(2)\n" \
  "ds_write_b32 %[ad], v163\n" \
  "v_cvt_pk_bf16_f32 v132, v104, v105\n" \
  "v_cvt_pk_bf16_f32 v133, v106, v107\n" \
  "v_cvt_pk_bf16_f32 v134, v108, v109\n" \
  "v_cvt_pk_bf16_f32 v135, v110, v111\n" \
  "v_mfma_f32_32x32x16_bf16 v[64:79], v[152:155], %[qa0], v[64:79]\n" \
  "v_exp_f32 v112, v112\n" \
  "v_exp_f32 v113, v113\n" \
  "v_exp_f32 v114, v114\n" \
  "v_exp_f32 v115, v115\n" \
  "v_mfma_f32_32x32x16_bf16 %[oa], v[148:151], v[132:135], %[oa]\n" \
  "v_exp_f32 v116, v116\n" \
  "v_exp_f32 v117, v117\n" \
  "v_exp_f32 v118, v118\n" \
  "v_exp_f32 v119, v119\n" \
  "v_add_u32 %[go], %[inc], %[go]\n" \
  "s_add_u32 %[nd], %[nd], 1\n" \
  "v_cvt_pk_bf16_f32 v128, v112, v113\n" \
  "v_cvt_pk_bf16_f32 v129, v114, v115\n" \
  "v_cvt_pk_bf16_f32 v130, v116, v117\n" \
  "v_cvt_pk_bf16_f32 v131, v118, v119\n" \
  "v_exp_f32 v120, v120\n" \
  "v_exp_f32 v121, v121\n" \
  "v_mfma_f32_32x32x16_bf16 %[ob], v[144:147], v[128:131], %[ob]\n" \
  "v_exp_f32 v122, v122\n" \
  "v_exp_f32 v123, v123\n" \
  "v_exp_f32 v124, v124\n" \
  "v_exp_f32 v125, v125\n" \
  "v_mfma_f32_32x32x16_bf16 v[80:95], v[152:155], %[qa1], v[80:95]\n" \
  "v_exp_f32 v126, v126\n" \
  "v_exp_f32 v127, v127\n" \
  "v_cvt_pk_bf16_f32 v132, v120, v121\n" \
  "v_cvt_pk_bf16_f32 v133, v122, v123\n" \
  "v_cvt_pk_bf16_f32 v134, v124, v125\n" \
  "v_cvt_pk_bf16_f32 v135, v126, v127\n" \
  "v_exp_f32 v64, v64\n" \
  "v_exp_f32 v65, v65\n" \
  "v_mfma_f32_32x32x16_bf16 %[ob], v[148:151], v[132:135], %[ob]\n" \
  "ds_read_b128 v[144:147], %[av] offset:3968\n" \
  "ds_read_b128 v[148:151], %[av] offset:4672\n" \
  "v_exp_f32 v66, v66\n" \
  "v_exp_f32 v67, v67\n" \
  "v_exp_f32 v68, v68\n" \
  "v_mfma_f32_32x32x4_2b_bf16 v[96:127], v[160:161], %[qb], 0 cbsz:1 abid:1\n" \
  "v_exp_f32 v69, v69\n" \
  "v_exp_f32 v70, v70\n" \
  "v_exp_f32 v71, v71\n" \
  "v_cvt_pk_bf16_f32 v128, v64, v65\n" \
  "v_cvt_pk_bf16_f32 v129, v66, v67\n" \
  "v_cvt_pk_bf16_f32 v130, v68, v69\n" \
  "v_cvt_pk_bf16_f32 v131, v70, v71\n" \
  "v_exp_f32 v72, v72\n" \
  "v_exp_f32 v73, v73\n" \
  "v_exp_f32 v74, v74\n" \
  "s_waitcnt lgkmcnt(2)\n" \
  "v_mfma_f32_32x32x16_bf16 %[oa], v[136:139], v[128:131], %[oa]\n" \
  "v_exp_f32 v75, v75\n" \
  "v_exp_f32 v76, v76\n" \
  "v_exp_f32 v77, v77\n" \
  "v_exp_f32 v78, v78\n" \
  "v_exp_f32 v79, v79\n" \
  "ds_read_b32 v162, %[af]\n" \
  "v_cvt_pk_bf16_f32 v132, v72, v73\n" \
  "v_cvt_pk_bf16_f32 v133, v74, v75\n" \
  "v_cvt_pk_bf16_f32 v134, v76, v77\n" \
  "v_cvt_pk_bf16_f32 v135, v78, v79\n" \
  "v_mfma_f32_32x32x16_bf16 v[96:111], v[156:159], %[qa0], v[96:111]\n" \
  "v_exp_f32 v80, v80\n" \
  "v_exp_f32 v81, v81\n" \
  "v_exp_f32 v82, v82\n" \
  "v_exp_f32 v83, v83\n" \
  "v_mfma_f32_32x32x16_bf16 %[oa], v[140:143], v[132:135], %[oa]\n" \
  "v_exp_f32 v84, v84\n" \
  "v_exp_f32 v85, v85\n" \
  "v_exp_f32 v86, v86\n" \
  "v_exp_f32 v87, v87\n" \
  "s_waitcnt lgkmcnt(0)\n" \
  "v_cmp_gt_u32 vcc, %[nd], v162\n" \
  "s_add_u32 m0, %[dst], 32256\n" \
  "s_cbranch_vccnz L_pollb2_%=\n" \
  "L_okb2_%=:\n" \
  "s_cmp_eq_u32 %[cnt], 1\n" \
  "s_cbranch_scc1 L_nob2_%=\n" \
  "global_load_lds_dwordx4 %[go], %[src] sc1\n" \
  "global_load_lds_dwordx4 %[go], %[src] offset:320 sc1\n" \
  "L_dnb2_%=:\n" \
  "ds_read_b64 v[160:161], %[ab] offset:12800\n" \
  "ds_read_b128 v[152:155], %[ak] offset:10752\n" \
  "v_cvt_pk_bf16_f32 v128, v80, v81\n" \
  "v_cvt_pk_bf16_f32 v129, v82, v83\n" \
  "v_cvt_pk_bf16_f32 v130, v84, v85\n" \
  "v_cvt_pk_bf16_f32 v131, v86, v87\n" \
  "v_exp_f32 v88, v88\n" \
  "v_exp_f32 v89, v89\n" \
  "v_mfma_f32_32x32x16_bf16 %[ob], v[136:139], v[128:131], %[ob]\n" \
  "v_exp_f32 v90, v90\n" \
  "v_exp_f32 v91, v91\n" \
  "v_exp_f32 v92, v92\n" \
  "v_exp_f32 v93, v93\n" \
  "v_mfma_f32_32x32x16_bf16 v[112:127], v[156:159], %[qa1], v[112:127]\n" \
  "v_exp_f32 v94, v94\n" \
  "v_exp_f32 v95, v95\n" \
  "v_cvt_pk_bf16_f32 v132, v88, v89\n" \
  "v_cvt_pk_bf16_f32 v133, v90, v91\n" \
  "v_cvt_pk_bf16_f32 v134, v92, v93\n" \
  "v_cvt_pk_bf16_f32 v135, v94, v95\n" \
  "v_exp_f32 v96, v96\n" \
  "v_exp_f32 v97, v97\n" \
  "s_waitcnt lgkmcnt(0)\n" \
  "v_mfma_f32_32x32x16_bf16 %[ob], v[140:143], v[132:135], %[ob]\n" \
  "ds_read_b128 v[136:139], %[av] offset:13312\n" \
  "ds_read_b128 v[140:143], %[av] offset:14016\n" \
  "ds_read_b128 v[156:159], %[ak] offset:11776\n" \
  "v_exp_f32 v98, v98\n" \
  "v_exp_f32 v99, v99\n" \
  "v_exp_f32 v100, v100\n" \
  "v_mfma_f32_32x32x4_2b_bf16 v[64:95], v[160:161], %[qb], 0 cbsz:1 abid:0\n" \
  "v_exp_f32 v101, v101\n" \
  "v_exp_f32 v102, v102\n" \
  "v_exp_f32 v103, v103\n" \
  "v_cvt_pk_bf16_f32 v128, v96, v97\n" \
  "v_cvt_pk_bf16_f32 v129, v98, v99\n" \
  "v_cvt_pk_bf16_f32 v130, v100, v101\n" \
  "v_cvt_pk_bf16_f32 v131, v102, v103\n" \
  "v_exp_f32 v104, v104\n" \
  "v_exp_f32 v105, v105\n" \
  "v_exp_f32 v106, v106\n" \
  "v_mfma_f32_32x32x16_bf16 %[oa], v[144:147], v[128:131], %[oa]\n" \
  "v_exp_f32 v107, v107\n" \
  "v_exp_f32 v108, v108\n" \
  "v_exp_f32 v109, v109\n" \
  "v_exp_f32 v110, v110\n" \
  "v_exp_f32 v111, v111\n" \
  "v_add_u32 v163, 1, v163\n" \
  "s_waitcnt vmcnt(2)\n" \
  "ds_write_b32 %[ad], v163\n" \
  "v_cvt_pk_bf16_f32 v132, v104, v105\n" \
  "v_cvt_pk_bf16_f32 v133, v106, v107\n" \
  "v_cvt_pk_bf16_f32 v134, v108, v109\n" \
  "v_cvt_pk_bf16_f32 v135, v110, v111\n" \
  "v_mfma_f32_32x32x16_bf16 v[64:79], v[152:155], %[qa0], v[64:79]\n" \
  "v_exp_f32 v112, v112\n" \
  "v_exp_f32 v113, v113\n" \
  "v_exp_f32 v114, v114\n" \
  "v_exp_f32 v115, v115\n" \
  "v_mfma_f32_32x32x16_bf16 %[oa], v[148:151], v[132:135], %[oa]\n" \
  "v_exp_f32 v116, v116\n" \
  "v_exp_f32 v117, v117\n" \
  "v_exp_f32 v118, v118\n" \
  "v_exp_f32 v119, v119\n" \
  "v_add_u32 %[go], %[inc], %[go]\n" \
  "s_add_u32 %[nd], %[nd], 1\n" \
  "v_cvt_pk_bf16_f32 v128, v112, v113\n" \
  "v_cvt_pk_bf16_f32 v129, v114, v115\n" \
  "v_cvt_pk_bf16_f32 v130, v116, v117\n" \
  "v_cvt_pk_bf16_f32 v131, v118, v119\n" \
  "v_exp_f32 v120, v120\n" \
  "v_exp_f32 v121, v121\n" \
  "v_mfma_f32_32x32x16_bf16 %[ob], v[144:147], v[128:131], %[ob]\n" \
  "v_exp_f32 v122, v122\n" \
  "v_exp_f32 v123, v123\n" \
  "v_exp_f32 v124, v124\n" \
  "v_exp_f32 v125, v125\n" \
  "v_mfma_f32_32x32x16_bf16 v[80:95], v[152:155], %[qa1], v[80:95]\n" \
  "v_exp_f32 v126, v126\n" \
  "v_exp_f32 v127, v127\n" \
  "v_cvt_pk_bf16_f32 v132, v120, v121\n" \
  "v_cvt_pk_bf16_f32 v133, v122, v123\n" \
  "v_cvt_pk_bf16_f32 v134, v124, v125\n" \
  "v_cvt_pk_bf16_f32 v135, v126, v127\n" \
  "v_exp_f32 v64, v64\n" \
  "v_exp_f32 v65, v65\n" \
  "v_mfma_f32_32x32x16_bf16 %[ob], v[148:151], v[132:135], %[ob]\n" \
  "ds_read_b128 v[144:147], %[av] offset:14720\n" \
  "ds_read_b128 v[148:151], %[av] offset:15424\n" \
  "v_exp_f32 v66, v66\n" \
  "v_exp_f32 v67, v67\n" \
  "v_exp_f32 v68, v68\n" \
  "v_mfma_f32_32x32x4_2b_bf16 v[96:127], v[160:161], %[qb], 0 cbsz:1 abid:1\n" \
  "v_exp_f32 v69, v69\n" \
  "v_exp_f32 v70, v70\n" \
  "v_exp_f32 v71, v71\n" \
  "v_cvt_pk_bf16_f32 v128, v64, v65\n" \
  "v_cvt_pk_bf16_f32 v129, v66, v67\n" \
  "v_cvt_pk_bf16_f32 v130, v68, v69\n" \
  "v_cvt_pk_bf16_f32 v131, v70, v71\n" \
  "v_exp_f32 v72, v72\n" \
  "v_exp_f32 v73, v73\n" \
  "v_exp_f32 v74, v74\n" \
  "s_waitcnt lgkmcnt(2)\n" \
  "v_mfma_f32_32x32x16_bf16 %[oa], v[136:139], v[128:131], %[oa]\n" \
  "v_exp_f32 v75, v75\n" \
  "v_exp_f32 v76, v76\n" \
  "v_exp_f32 v77, v77\n" \
  "v_exp_f32 v78, v78\n" \
  "v_exp_f32 v79, v79\n" \
  "ds_read_b32 v162, %[af]\n" \
  "v_cvt_pk_bf16_f32 v132, v72, v73\n" \
  "v_cvt_pk_bf16_f32 v133, v74, v75\n" \
  "v_cvt_pk_bf16_f32 v134, v76, v77\n" \
  "v_cvt_pk_bf16_f32 v135, v78, v79\n" \
  "v_mfma_f32_32x32x16_bf16 v[96:111], v[156:159], %[qa0], v[96:111]\n" \
  "v_exp_f32 v80, v80\n" \
  "v_exp_f32 v81, v81\n" \
  "v_exp_f32 v82, v82\n" \
  "v_exp_f32 v83, v83\n" \
  "v_mfma_f32_32x32x16_bf16 %[oa], v[140:143], v[132:135], %[oa]\n" \
  "v_exp_f32 v84, v84\n" \
  "v_exp_f32 v85, v85\n" \
  "v_exp_f32 v86, v86\n" \
  "v_exp_f32 v87, v87\n" \
  "s_waitcnt lgkmcnt(0)\n" \
  "v_cmp_gt_u32 vcc, %[nd], v162\n" \
  "s_add_u32 m0, %[dst], 0\n" \
  "s_cbranch_vccnz L_pollb3_%=\n" \
  "L_okb3_%=:\n" \
  "s_cmp_eq_u32 %[cnt], 1\n" \
  "s_cbranch_scc1 L_nob3_%=\n" \
  "global_load_lds_dwordx4 %[go], %[src] sc1\n" \
  "global_load_lds_dwordx4 %[go], %[src] offset:320 sc1\n" \
  "L_dnb3_%=:\n" \
  "ds_read_b64 v[160:161], %[ab] offset:23552\n" \
  "ds_read_b128 v[152:155], %[ak] offset:21504\n" \
  "v_cvt_pk_bf16_f32 v128, v80, v81\n" \
  "v_cvt_pk_bf16_f32 v129, v82, v83\n" \
  "v_cvt_pk_bf16_f32 v130, v84, v85\n" \
  "v_cvt_pk_bf16_f32 v131, v86, v87\n" \
  "v_exp_f32 v88, v88\n" \
  "v_exp_f32 v89, v89\n" \
  "v_mfma_f32_32x32x16_bf16 %[ob], v[136:139], v[128:131], %[ob]\n" \
  "v_exp_f32 v90, v90\n" \
  "v_exp_f32 v91, v91\n" \
  "v_exp_f32 v92, v92\n" \
  "v_exp_f32 v93, v93\n" \
  "v_mfma_f32_32x32x16_bf16 v[112:127], v[156:159], %[qa1], v[112:127]\n" \
  "v_exp_f32 v94, v94\n" \
  "v_exp_f32 v95, v95\n" \
  "v_cvt_pk_bf16_f32 v132, v88, v89\n" \
  "v_cvt_pk_bf16_f32 v133, v90, v91\n" \
  "v_cvt_pk_bf16_f32 v134, v92, v93\n" \
  "v_cvt_pk_bf16_f32 v135, v94, v95\n" \
  "v_exp_f32 v96, v96\n" \
  "v_exp_f32 v97, v97\n" \
  "s_waitcnt lgkmcnt(0)\n" \
  "v_mfma_f32_32x32x16_bf16 %[ob], v[140:143], v[132:135], %[ob]\n" \
  "ds_read_b128 v[136:139], %[av] offset:24064\n" \
  "ds_read_b128 v[140:143], %[av] offset:24768\n" \
  "ds_read_b128 v[156:159], %[ak] offset:22528\n" \
  "v_exp_f32 v98, v98\n" \
  "v_exp_f32 v99, v99\n" \
  "v_exp_f32 v100, v100\n" \
  "v_mfma_f32_32x32x4_2b_bf16 v[64:95], v[160:161], %[qb], 0 cbsz:1 abid:0\n" \
  "v_exp_f32 v101, v101\n" \
  "v_exp_f32 v102, v102\n" \
  "v_exp_f32 v103, v103\n" \
  "v_cvt_pk_bf16_f32 v128, v96, v97\n" \
  "v_cvt_pk_bf16_f32 v129, v98, v99\n" \
  "v_cvt_pk_bf16_f32 v130, v100, v101\n" \
  "v_cvt_pk_bf16_f32 v131, v102, v103\n" \
  "v_exp_f32 v104, v104\n" \
  "v_exp_f32 v105, v105\n" \
  "v_exp_f32 v106, v106\n" \
  "v_mfma_f32_32x32x16_bf16 %[oa], v[144:147], v[128:131], %[oa]\n" \
  "v_exp_f32 v107, v107\n" \
  "v_exp_f32 v108, v108\n" \
  "v_exp_f32 v109, v109\n" \
  "v_exp_f32 v110, v110\n" \
  "v_exp_f32 v111, v111\n" \
  "v_add_u32 v163, 1, v163\n" \
  "s_waitcnt vmcnt(2)\n" \
  "ds_write_b32 %[ad], v163\n" \
  "v_cvt_pk_bf16_f32 v132, v104, v105\n" \
  "v_cvt_pk_bf16_f32 v133, v106, v107\n" \
  "v_cvt_pk_bf16_f32 v134, v108, v109\n" \
  "v_cvt_pk_bf16_f32 v135, v110, v111\n" \
  "v_mfma_f32_32x32x16_bf16 v[64:79], v[152:155], %[qa0], v[64:79]\n" \
  "v_exp_f32 v112, v112\n" \
  "v_exp_f32 v113, v113\n" \
  "v_exp_f32 v114, v114\n" \
  "v_exp_f32 v115, v115\n" \
  "v_mfma_f32_32x32x16_bf16 %[oa], v[148:151], v[132:135], %[oa]\n" \
  "v_exp_f32 v116, v116\n" \
  "v_exp_f32 v117, v117\n" \
  "v_exp_f32 v118, v118\n" \
  "v_exp_f32 v119, v119\n" \
  "v_add_u32 %[go], %[inc], %[go]\n" \
  "s_add_u32 %[nd], %[nd], 1\n" \
  "v_cvt_pk_bf16_f32 v128, v112, v113\n" \
  "v_cvt_pk_bf16_f32 v129, v114, v115\n" \
  "v_cvt_pk_bf16_f32 v130, v116, v117\n" \
  "v_cvt_pk_bf16_f32 v131, v118, v119\n" \
  "v_exp_f32 v120, v120\n" \
  "v_exp_f32 v121, v121\n" \
  "v_mfma_f32_32x32x16_bf16 %[ob], v[144:147], v[128:131], %[ob]\n" \
  "v_exp_f32 v122, v122\n" \
  "v_exp_f32 v123, v123\n" \
  "v_exp_f32 v124, v124\n" \
  "v_exp_f32 v125, v125\n" \
  "v_mfma_f32_32x32x16_bf16 v[80:95], v[152:155], %[qa1], v[80:95]\n" \
  "v_exp_f32 v126, v126\n" \
  "v_exp_f32 v127, v127\n" \
  "v_cvt_pk_bf16_f32 v132, v120, v121\n" \
  "v_cvt_pk_bf16_f32 v133, v122, v123\n" \
  "v_cvt_pk_bf16_f32 v134, v124, v125\n" \
  "v_cvt_pk_bf16_f32 v135, v126, v127\n" \
  "s_sub_u32 %[cnt], %[cnt], 1\n" \
  "s_cmp_lg_u32 %[cnt], 0\n" \
  "s_cbranch_scc1 L_loopb%=\n" \
  "s_nop 1\n" \
  "v_mfma_f32_32x32x16_bf16 %[ob], v[148:151], v[132:135], %[ob]\n" \
  "s_waitcnt lgkmcnt(0)\n" \
  "s_branch L_end%=\n" \
  "L_pollb0_%=:\n" \
  "s_sub_u32 %[cap], %[cap], 1\n" \
  "s_cbranch_scc1 L_okb0_%=\n" \
  "s_sleep 1\n" \
  "ds_read_b32 v162, %[af]\n" \
  "s_waitcnt lgkmcnt(0)\n" \
  "v_cmp_gt_u32 vcc, %[nd], v162\n" \
  "s_cbranch_vccnz L_pollb0_%=\n" \
  "s_branch L_okb0_%=\n" \
  "L_nob1_%=:\n" \
  "s_waitcnt vmcnt(0)\n" \
  "s_branch L_dnb1_%=\n" \
  "L_pollb1_%=:\n" \
  "s_sub_u32 %[cap], %[cap], 1\n" \
  "s_cbranch_scc1 L_okb1_%=\n" \
  "s_sleep 1\n" \
  "ds_read_b32 v162, %[af]\n" \
  "s_waitcnt lgkmcnt(0)\n" \
  "v_cmp_gt_u32 vcc, %[nd], v162\n" \
  "s_cbranch_vccnz L_pollb1_%=\n" \
  "s_branch L_okb1_%=\n" \
  "L_nob2_%=:\n" \
  "s_waitcnt vmcnt(0)\n" \
  "s_branch L_dnb2_%=\n" \
  "L_pollb2_%=:\n" \
  "s_sub_u32 %[cap], %[cap], 1\n" \
  "s_cbranch_scc1 L_okb2_%=\n" \
  "s_sleep 1\n" \
  "ds_read_b32 v162, %[af]\n" \
  "s_waitcnt lgkmcnt(0)\n" \
  "v_cmp_gt_u32 vcc, %[nd], v162\n" \
  "s_cbranch_vccnz L_pollb2_%=\n" \
  "s_branch L_okb2_%=\n" \
  "L_nob3_%=:\n" \
  "s_waitcnt vmcnt(0)\n" \
  "s_branch L_dnb3_%=\n" \
  "L_pollb3_%=:\n" \
  "s_sub_u32 %[cap], %[cap], 1\n" \
  "s_cbranch_scc1 L_okb3_%=\n" \
  "s_sleep 1\n" \
  "ds_read_b32 v162, %[af]\n" \
  "s_waitcnt lgkmcnt(0)\n" \
  "v_cmp_gt_u32 vcc, %[nd], v162\n" \
  "s_cbranch_vccnz L_pollb3_%=\n" \
  "s_branch L_okb3_%=\n" \
  "L_end%=:\n" \
  "s_nop 15\n" \
  ""
#define ATTN_CLOB "v64","v65","v66","v67","v68","v69","v70","v71","v72","v73","v74","v75","v76","v77","v78","v79","v80","v81","v82","v83","v84","v85","v86","v87","v88","v89","v90","v91","v92","v93","v94","v95","v96","v97","v98","v99","v100","v101","v102","v103","v104","v105","v106","v107","v108","v109","v110","v111","v112","v113","v114","v115","v116","v117","v118","v119","v120","v121","v122","v123","v124","v125","v126","v127","v128","v129","v130","v131","v132","v133","v134","v135","v136","v137","v138","v139","v140","v141","v142","v143","v144","v145","v146","v147","v148","v149","v150","v151","v152","v153","v154","v155","v156","v157","v158","v159","v160","v161","v162","v163"

constexpr int SEQ = 2048, DM = 640, ROWB = DM * 2;
constexpr int GRP_B = 5376, SLOT_B = 2 * GRP_B, VS_B = 704;
constexpr int UT_B = 4096;
constexpr unsigned FL = 128;
constexpr unsigned WS_EPOCH = 0, WS_FKV = FL, WS_FU = WS_FKV + 256 * FL, WS_FA = WS_FU + 256 * FL, WS_KV = WS_FA + 768 * FL + 128;
constexpr unsigned WS_UT = WS_KV + 32 * GRP_B + 1024;
constexpr unsigned WS_AT = WS_UT + DM * UT_B;
constexpr unsigned WS_O = WS_AT + SEQ * DM * 2;
constexpr int L_W = 0, L_WKV = 102400, L_GW = L_WKV + 25600, L_TAB = L_GW + 1280, TAB_B = 32 * 272, L_KVX = L_TAB + 3 * TAB_B;
constexpr int L_QP = 0, QP_B = 51200, QP_ROW = 336, L_RING = L_WKV - 2 * SLOT_B, L_U = 25600, U_B = 512 + 4096, L_CP = L_U + 3 * U_B;
constexpr int L_WO3 = L_RING + 4 * SLOT_B, L_AT = L_WO3 + 25600, L_CTR = L_AT + 10240, L_GATE = L_CTR + 256, L_US2 = L_GATE + 512, L_END = L_US2 + 2048, L_US0 = L_WO3, L_US1 = L_KVX;
static_assert(L_US0 + 4096 <= L_GW && L_KVX + 4096 <= L_CTR && L_END <= 163840 && L_CP + 2 * 8192 <= 56320 && 76800 <= L_RING, "LDS map");
enum { C_TAG, C_XD, C_WQ, C_KVX, C_WQL, C_U, C_QW, C_READY, C_CONV, C_WO, C_AT, C_H, C_BUF = 12, C_PART = 16, C_PAIR = 24, C_DONE = 32, C_N = 48 };
constexpr float QSCALE = 1.4426950408889634f * 0.22360679774997896f;
constexpr unsigned SPIN_CAP = 1u << 16, LSPIN_CAP = 1u << 20;

#define MFMA32(a, b, c) __builtin_amdgcn_mfma_f32_32x32x16_bf16(__builtin_bit_cast(bf16x8, a), __builtin_bit_cast(bf16x8, b), c, 0, 0, 0)
#define MFMA16(a, b, c) __builtin_amdgcn_mfma_f32_16x16x32_bf16(__builtin_bit_cast(bf16x8, a), __builtin_bit_cast(bf16x8, b), c, 0, 0, 0)
#define RAWBAR() asm volatile("s_waitcnt lgkmcnt(0)\n\ts_barrier" ::: "memory")
#define LGWAIT() asm volatile("s_waitcnt lgkmcnt(0)" ::: "memory")
#define VMWAIT(n) asm volatile("s_waitcnt vmcnt(%0)" :: "i"(n) : "memory")
#define PIN2(a, b) asm volatile("" : "+v"(a), "+v"(b))
#define PIN4(a, b, c, d) asm volatile("" : "+v"(a), "+v"(b), "+v"(c), "+v"(d))
#define XPRIO 3
#define PACE(y) asm volatile("s_sleep %1" : "+v"(y) : "i"(CONVSLEEP))
#define APRIO 2
#define KPRIO2 KPRIO
#define KPRIO 2
#define SLOWOK true
#define OSLEEP 3
#define ODELAY 4
#define CVPRIO 2
#define OST8(p) *(u32x2*)(p) =
#define OST4(p) *(unsigned*)(p) =
#define OSTE
#define PPRIO 3
#define PFPRIO 0
#define PSLEEP 3
#define SBAR() __builtin_amdgcn_sched_barrier(0)
#define STAMP(i)

__device__ __forceinline__ unsigned cvtpk(float lo, float hi) { f32x2 v = {lo, hi}; return __builtin_bit_cast(unsigned, __builtin_convertvector(v, bf16x2)); }
__device__ __forceinline__ float bf_lo(unsigned w) { return __builtin_bit_cast(float, w << 16); }
__device__ __forceinline__ float bf_hi(unsigned w) { return __builtin_bit_cast(float, w & 0xffff0000u); }
__device__ __forceinline__ float bf1(u16 w) { return __builtin_bit_cast(float, (unsigned)w << 16); }
__device__ __forceinline__ void glds16(const void* base, unsigned voff, unsigned lds) {
    asm volatile("s_mov_b32 m0, %2\n\ts_nop 0\n\tglobal_load_lds_dwordx4 %0, %1" :: "v"(voff), "s"(base), "s"(lds) : "memory");
}
__device__ __forceinline__ void glds16_dev(const void* base, unsigned voff, unsigned lds) {
    asm volatile("s_mov_b32 m0, %2\n\ts_nop 0\n\tglobal_load_lds_dwordx4 %0, %1 sc1" :: "v"(voff), "s"(base), "s"(lds) : "memory");
}
__device__ __forceinline__ void glds64_dev(const void* base, unsigned voff, unsigned lds) {
    asm volatile("s_mov_b32 m0, %2\n\ts_nop 0\n\tglobal_load_lds_dwordx4 %0, %1 sc1\n\tglobal_load_lds_dwordx4 %0, %1 offset:1024 sc1\n\t"
                 "global_load_lds_dwordx4 %0, %1 offset:2048 sc1\n\tglobal_load_lds_dwordx4 %0, %1 offset:3072 sc1" :: "v"(voff), "s"(base), "s"(lds) : "memory");
}
__device__ __forceinline__ void glds64(const void* base, unsigned voff, unsigned lds) {
    asm volatile("s_mov_b32 m0, %2\n\ts_nop 0\n\tglobal_load_lds_dwordx4 %0, %1\n\tglobal_load_lds_dwordx4 %0, %1 offset:1024\n\t"
                 "global_load_lds_dwordx4 %0, %1 offset:2048\n\tglobal_load_lds_dwordx4 %0, %1 offset:3072" :: "v"(voff), "s"(base), "s"(lds) : "memory");
}
#define WPAIR "s_mov_b32 m0, %2\n\tv_add_u32 %1, 0x64000, %0\n\tglobal_load_lds_dwordx4 %0, %3\n\ts_add_u32 m0, %2, 0xc800\n\tv_add_u32 %0, 0xa000, %0\n\t" \
              "global_load_lds_dwordx4 %1, %3\n\ts_add_u32 %2, %2, 0x1400\n\t"
template <int OFF> __device__ __forceinline__ u32x4 gld16(const void* base, unsigned voff) { u32x4 r; asm volatile("global_load_dwordx4 %0, %1, %2 offset:%3" : "=v"(r) : "v"(voff), "s"(base), "i"(OFF) : "memory"); return r; }
__device__ __forceinline__ u32x4 gld16_dev(const void* base, unsigned voff) { u32x4 r; asm volatile("global_load_dwordx4 %0, %1, %2 sc1" : "=v"(r) : "v"(voff), "s"(base) : "memory"); return r; }
__device__ __forceinline__ void st16_dev(void* base, unsigned voff, u32x4 v) { asm volatile("global_store_dwordx4 %0, %1, %2 sc1\n\ts_nop 1" :: "v"(voff), "v"(v), "s"(base) : "memory"); }
__device__ __forceinline__ void st8_dev(void* base, unsigned voff, u32x2 v) { asm volatile("global_store_dwordx2 %0, %1, %2 sc1" :: "v"(voff), "v"(v), "s"(base) : "memory"); }
__device__ __forceinline__ void st4_dev(void* base, unsigned voff, unsigned v) { asm volatile("global_store_dword %0, %1, %2 sc1" :: "v"(voff), "v"(v), "s"(base) : "memory"); }
__device__ __forceinline__ void poll4(const void* base, unsigned voff, unsigned tag) {
#pragma unroll 1
    for (unsigned n = 0; n < SPIN_CAP; ++n) {
        unsigned v; asm volatile("global_load_dword %0, %1, %2 sc1\n\ts_waitcnt vmcnt(0)" : "=v"(v) : "v"(voff), "s"(base) : "memory");
        if (__all(v == tag)) break;
        __builtin_amdgcn_s_sleep(3);
    }
}
__device__ __forceinline__ u32x2 trrd(lds_ptr p) { return __builtin_bit_cast(u32x2, __builtin_amdgcn_ds_read_tr16_b64_v4i16((LAS s16x4*)p)); }
__device__ __forceinline__ u32x4 trfrag(lds_ptr p0, lds_ptr p1, int off) { u32x2 a = trrd(p0 + off), b = trrd(p1 + off); return (u32x4){a.x, a.y, b.x, b.y}; }
__device__ __forceinline__ void arrive(lds_u32 c, int lane) { LGWAIT(); if (lane == 0) __hip_atomic_fetch_add(c, 1u, __ATOMIC_RELAXED, __HIP_MEMORY_SCOPE_WORKGROUP); }
__device__ __forceinline__ unsigned arrive_rtn(lds_u32 c, int lane) {
    LGWAIT(); unsigned r = 0; if (lane == 0) r = __hip_atomic_fetch_add(c, 1u, __ATOMIC_RELAXED, __HIP_MEMORY_SCOPE_WORKGROUP);
    return (unsigned)__builtin_amdgcn_readfirstlane(r);
}
__device__ __forceinline__ void post(lds_u32 c, unsigned v) { LGWAIT(); __hip_atomic_store(c, v, __ATOMIC_RELAXED, __HIP_MEMORY_SCOPE_WORKGROUP); }
template <int SL = 1>
__device__ __forceinline__ unsigned await(lds_u32 c, unsigned target) {
    unsigned v, n = 0;
    while ((v = (unsigned)__builtin_amdgcn_readfirstlane(__hip_atomic_load(c, __ATOMIC_RELAXED, __HIP_MEMORY_SCOPE_WORKGROUP))) < target && ++n < LSPIN_CAP) __builtin_amdgcn_s_sleep(SL);
    asm volatile("" ::: "memory");
    return v;
}
template <int PR>
__device__ __forceinline__ void await_lo(lds_u32 c, unsigned target) {
    if ((unsigned)__builtin_amdgcn_readfirstlane(__hip_atomic_load(c, __ATOMIC_RELAXED, __HIP_MEMORY_SCOPE_WORKGROUP)) < target) {
        __builtin_amdgcn_s_setprio(0); await<0>(c, target); __builtin_amdgcn_s_setprio(PR);
    }
    asm volatile("" ::: "memory");
}
__device__ __forceinline__ void await2(lds_u32 a, unsigned ta, lds_u32 b, unsigned tb) {
    unsigned n = 0;
    while (true) {
        const unsigned x = __hip_atomic_load(a, __ATOMIC_RELAXED, __HIP_MEMORY_SCOPE_WORKGROUP), y = __hip_atomic_load(b, __ATOMIC_RELAXED, __HIP_MEMORY_SCOPE_WORKGROUP);
        if (__all(x >= ta && y >= tb) || ++n >= LSPIN_CAP) break;
    }
    asm volatile("" ::: "memory");
}
template <int SL = 1>
__device__ __forceinline__ void await8(lds_u32 c, unsigned target, int lane) {
    unsigned n = 0;
    while (!__all(__hip_atomic_load(c + (lane & 7), __ATOMIC_RELAXED, __HIP_MEMORY_SCOPE_WORKGROUP) >= target) && ++n < LSPIN_CAP) __builtin_amdgcn_s_sleep(SL);
    asm volatile("" ::: "memory");
}

__device__ __forceinline__ int wrow_of(int pos, int lane, int& c) { const int s = 64 * pos + lane, r = s / 10; c = s - 10 * r; return r ^ (((r >> 3) & 1) << 2); }
__device__ __forceinline__ int wo_row_ab(int r) { return 20 * (r >> 4) + 4 * ((r >> 3) & 1) + (r & 3) + 8 * ((r >> 2) & 1); }
__device__ __forceinline__ int wo_row_c(int r) { return 20 * (r >> 2) + 16 + (r & 3); }

struct cplx2 { f32x2 re, im; };
__device__ __forceinline__ cplx2 cmul(cplx2 a, cplx2 w) {
    f32x2 re, im;
    asm("v_pk_mul_f32 %0, %3, %5\n\tv_pk_mul_f32 %1, %3, %4\n\tv_pk_fma_f32 %0, %2, %4, %0 neg_lo:[0,0,1] neg_hi:[0,0,1]\n\tv_pk_fma_f32 %1, %2, %5, %1"
        : "=&v"(re), "=&v"(im) : "v"(a.re), "v"(a.im), "v"(w.re), "v"(w.im));
    return {re, im};
}
__device__ __forceinline__ cplx2 cmulc(cplx2 a, cplx2 w) {
    f32x2 re, im;
    asm("v_pk_mul_f32 %0, %3, %5\n\tv_pk_mul_f32 %1, %3, %4\n\tv_pk_fma_f32 %0, %2, %4, %0\n\tv_pk_fma_f32 %1, %2, %5, %1 neg_lo:[1,0,0] neg_hi:[1,0,0]"
        : "=&v"(re), "=&v"(im) : "v"(a.re), "v"(a.im), "v"(w.re), "v"(w.im));
    return {re, im};
}
__device__ __forceinline__ float opq(float v) { asm("" : "+v"(v)); return v; }
__device__ __forceinline__ cplx2 cexpm(f32x2 lr, f32x2 rev, float m) {
    const float m0 = __builtin_amdgcn_exp2f(lr.x * m), a0 = __builtin_amdgcn_fractf(rev.x * m), m1 = __builtin_amdgcn_exp2f(lr.y * m), a1 = __builtin_amdgcn_fractf(rev.y * m);
    return {(f32x2){opq(m0 * __builtin_amdgcn_cosf(a0)), opq(m1 * __builtin_amdgcn_cosf(a1))}, (f32x2){opq(m0 * __builtin_amdgcn_sinf(a0)), opq(m1 * __builtin_amdgcn_sinf(a1))}};
}

template <class F>
__device__ __forceinline__ f32x16 kgen_wave(int lane, lds_ptr tab, unsigned wa, unsigned wi, unsigned wbr, unsigned wbi, unsigned wcr, unsigned wci, unsigned ldt, unsigned dsk16, F mid) {
    const float dt = bf_lo(cvtpk(expf(bf_lo(ldt)), 0.f));
    const float dsk = bf_lo(dsk16);
    const int rho = lane & 31, h = lane >> 5;
    const unsigned long long mp = rho < 8 ? 0x0a09080803020100ull : rho < 16 ? 0x0c0b0a0a07060504ull : rho < 24 ? 0x121211100f0f0e0dull : 0x1a19161518171413ull;
    const int xr = (int)((mp >> (8 * (rho & 7))) & 31);
    const lds_ptr pa = tab + xr * 272 + 16 * h, pb = tab + rho * 272 + 16 * h;
    lds_u32 wr = (lds_u32)(tab + lane * 4);
    const f32x2 lr = {opq((dt * bf_lo(wa)) * 1.4426950408889634f), opq((dt * bf_hi(wa)) * 1.4426950408889634f)};
    const f32x2 rev = {opq((dt * bf_lo(wi)) * 0.15915494309189535f), opq((dt * bf_hi(wi)) * 0.15915494309189535f)};
    const cplx2 w1 = cexpm(lr, rev, 1.f), w8 = cexpm(lr, rev, 8.f), w256 = cexpm(lr, rev, 256.f), w2 = cmul(w1, w1), w4 = cmul(w2, w2);
    const cplx2 cb = cmulc({(f32x2){bf_lo(wcr), bf_hi(wcr)}, (f32x2){opq(-bf_lo(wci)), opq(-bf_hi(wci))}},
                           {(f32x2){opq(dt * bf_lo(wbr)), opq(dt * bf_hi(wbr))}, (f32x2){opq(dt * bf_lo(wbi)), opq(dt * bf_hi(wbi))}});
    unsigned hold[32];
#define PUT(r, v) do { wr[(r) * 68] = cvtpk((v).re.x, (v).im.x); hold[r] = cvtpk((v).re.y, (v).im.y); } while (0)
    cplx2 x = cb; PUT(0, x);
#pragma unroll
    for (int i = 1; i < 8; ++i) { x = cmulc(x, w1); PUT(i, x); }
    cplx2 y = cmulc(cb, w256); PUT(8, y); x = y;
#pragma unroll
    for (int i = 9; i < 13; ++i) { x = cmulc(x, w2); PUT(i, x); }
    y = cmulc(y, w256); PUT(13, y); x = cmulc(y, w4); PUT(14, x); x = cmulc(x, w4); PUT(15, x);
    y = cmulc(y, w256); PUT(16, y); x = cmulc(y, w4); PUT(17, x); x = cmulc(x, w4); PUT(18, x);
#pragma unroll
    for (int i = 0; i < 4; ++i) { y = cmulc(y, w256); PUT(19 + 2 * i, y); x = cmulc(y, w8); PUT(20 + 2 * i, x); }
    u32x4 ae[8], ao[8];
#pragma unroll
    for (int s = 0; s < 8; ++s) ae[s] = *(const LAS u32x4*)(pa + 32 * s);
#pragma unroll
    for (int r = 0; r < 27; ++r) wr[r * 68] = hold[r];
#pragma unroll
    for (int s = 0; s < 8; ++s) ao[s] = *(const LAS u32x4*)(pa + 32 * s);
    mid();
    cplx2 e0 = cmul(w8, w8), e1 = w8; const cplx2 w16 = e0;
    wr[0] = 0x00003f80u; PUT(1, e1); PUT(2, e0);
#pragma unroll
    for (int j = 3; j < 31; j += 2) { e1 = cmul(e1, w16); PUT(j, e1); e0 = cmul(e0, w16); PUT(j + 1, e0); }
    e1 = cmul(e1, w16); PUT(31, e1);
#undef PUT
    f32x16 acc = {0};
    u32x4 bf[8];
#pragma unroll
    for (int s = 0; s < 8; ++s) bf[s] = *(const LAS u32x4*)(pb + 32 * s);
#pragma unroll
    for (int r = 1; r < 32; ++r) wr[r * 68] = hold[r];
#pragma unroll
    for (int s = 0; s < 8; ++s) { acc = MFMA32(ae[s], bf[s], acc); }
#pragma unroll
    for (int s = 0; s < 8; ++s) bf[s] = *(const LAS u32x4*)(pb + 32 * s);
#pragma unroll
    for (int s = 0; s < 8; ++s) { acc = MFMA32(ao[s], bf[s], acc); }
    if (lane == 0) acc[0] += dsk;
    return acc;
}
__device__ __forceinline__ void kexpand(const f32x16& acc, int lane, lds_ptr kr) {
    typedef LAS u32x4* V;
    const int j = lane & 31, h = lane >> 5;
    const float k7 = acc[7];
    const float prev = __builtin_bit_cast(float, __builtin_amdgcn_update_dpp(0, __builtin_bit_cast(int, k7), 0x138, 0xf, 0xf, false));
    const lds_ptr ca = kr + (1008 - 16 * j - 512 * h), cb = ca + 4160;
    float k[16];
#pragma unroll
    for (int i = 0; i < 16; ++i) { k[i] = acc[i]; asm("" : "+v"(k[i])); }
    *(V)(ca + 3072) = (u32x4){cvtpk(k[7], k[6]), cvtpk(k[5], k[4]), cvtpk(k[3], k[2]), cvtpk(k[1], k[0])};
    *(V)(cb + 3072) = (u32x4){cvtpk(k[6], k[5]), cvtpk(k[4], k[3]), cvtpk(k[2], k[1]), cvtpk(k[0], prev)};
    { const float a = k[8], b = k[9], n = k[10]; const unsigned aa = cvtpk(a, a), bb = cvtpk(b, b);
      *(V)(ca + 2048) = (u32x4){cvtpk(n, n), bb, cvtpk(b, a), aa};
      *(V)(cb + 2048) = (u32x4){cvtpk(n, b), bb, aa, aa}; }
#pragma unroll
    for (int s = 0; s < 2; ++s) { const float a = k[12 + 2 * s], n = k[13 + 2 * s], c = (j & 1) ? n : a; const unsigned aa = cvtpk(a, a), nn = cvtpk(n, n);
      *(V)(ca + 1024 - 1024 * s) = (u32x4){nn, cvtpk(n, c), aa, aa};
      *(V)(cb + 1024 - 1024 * s) = (u32x4){nn, cvtpk(c, a), aa, aa}; }
    if (lane < 8) *(V)(kr + (4096 + (lane >> 2) * 4160 + (lane & 3) * 16)) = (u32x4){0, 0, 0, 0};
}

template <int K0, int K1>
__device__ __forceinline__ void gemm_steps(f32x4 (&acc)[5], const u32x4 (&xf)[10], lds_ptr p0, lds_ptr p1) {
    u32x4 cur[5], nxt[5];
#pragma unroll
    for (int c = 0; c < 5; ++c) cur[c] = trfrag(p0, p1, K0 * 5120 + 32 * c);
#pragma unroll
    for (int i = K0; i < K1; ++i) {
#pragma unroll
        for (int c = 0; c < 5; ++c) {
            if (i + 1 < K1) nxt[c] = trfrag(p0, p1, (i + 1) * 5120 + 32 * c);
            SBAR();
            acc[c] = MFMA16(cur[c], xf[i], acc[c]);
            asm volatile("" : "+v"(acc[c]));
            SBAR();
        }
#pragma unroll
        for (int c = 0; c < 5; ++c) cur[c] = nxt[c];
    }
}

template <int K0, int K1>
__device__ __forceinline__ void gemm_run(f32x4 (&acc)[5], const u32x4 (&xf)[10], u32x4 (&wf)[4][5], lds_ptr p0, lds_ptr p1, int sh) {
    const lds_ptr q0 = p0 + sh, q1 = p1 + sh;
#pragma unroll
    for (int i = K0; i < K1; ++i)
#pragma unroll
        for (int c = 0; c < 5; ++c) {
            acc[c] = MFMA16(wf[i & 3][c], xf[i], acc[c]);
            asm volatile("" : "+v"(acc[c]));
            SBAR();
            if (i + 4 < 10) wf[i & 3][c] = i + 4 < 5 ? trfrag(p0, p1, (i + 4) * 5120 + 32 * c) : trfrag(q0, q1, (i + 4) * 5120 + 32 * c);
            SBAR();
        }
}

template <int TILES>
__device__ __forceinline__ void conv_class(f32x4 (&y)[8], lds_ptr fa, lds_ptr ga) {
    constexpr int NF = (TILES & 0x80) ? 8 : 6;
    u32x4 A[NF], B[NF];
#pragma unroll
    for (int m = 0; m < NF; ++m) { const LAS unsigned* q = (const LAS unsigned*)(fa + 3584 - 512 * m); A[m] = (u32x4){q[0], q[1], q[2], q[3]}; B[m] = *(const LAS u32x4*)(ga + 512 * m); }
    SBAR();
#pragma unroll
    for (int m = 0; m < NF; ++m)
#pragma unroll
        for (int n = 0; n < NF - m; ++n)
            if ((TILES >> (m + n)) & 1) { y[m + n] = MFMA16(A[m], B[n], y[m + n]); PACE(y[m + n]); }
}

__device__ __forceinline__ f32x16 attn_slow(const char* kv, u32x4 qa, u32x2 q4, int lane) {
    const int hi = lane >> 5, r32 = lane & 31, vrow = r32 <= 20 ? r32 : 21;
    const s16x4 qb = __builtin_bit_cast(s16x4, hi ? (u32x2){0u, 0u} : q4);
    float m = -INFINITY; f32x16 o = {0};
    __builtin_amdgcn_fence(__ATOMIC_ACQUIRE, "agent");
#pragma unroll 1
    for (int pass = 0; pass < 2; ++pass) {
#pragma unroll 1
        for (int t = 0; t < 64; ++t) {
            const char* gb = kv + (t >> 1) * GRP_B; const int tt = t & 1;
            const u32x4 ka = *(const u32x4*)(gb + tt * 1024 + lane * 16);
            u32x2 kb = *(const u32x2*)(gb + 2048 + (tt * 32 + r32) * 8); if (hi) kb = (u32x2){0u, 0u};
            f32x16 s = __builtin_amdgcn_mfma_f32_32x32x8bf16_1k(__builtin_bit_cast(s16x4, kb), qb, ((f32x16){0}), 0, 0, 0);
            s = MFMA32(ka, qa, s);
            if (pass == 0) {
#pragma unroll
                for (int i = 0; i < 16; ++i) m = (s[i] > m || s[i] != s[i]) ? s[i] : m;
            } else {
                u32x4 p0, p1;
#pragma unroll
                for (int i = 0; i < 4; ++i) { p0[i] = cvtpk(__builtin_amdgcn_exp2f(s[2 * i] - m), __builtin_amdgcn_exp2f(s[2 * i + 1] - m));
                                              p1[i] = cvtpk(__builtin_amdgcn_exp2f(s[8 + 2 * i] - m), __builtin_amdgcn_exp2f(s[9 + 2 * i] - m)); }
                const char* vb = gb + 2560 + tt * 2 * VS_B + (hi * 22 + vrow) * 16;
                o = MFMA32(*(const u32x4*)vb, p0, o); o = MFMA32(*(const u32x4*)(vb + VS_B), p1, o);
            }
        }
        if (pass == 0) { const float m2 = __shfl_xor(m, 32); m = (m2 > m || m2 != m2) ? m2 : m; }
    }
    return o;
}

#define OKS(h, i) ((i) < 8 ? 8 * (h) + (((i) + g) & 7) : 8 + 2 * (h) + (i))
__global__ __launch_bounds__(768) void fused(const u16* __restrict__ x, const u16* __restrict__ are, const u16* __restrict__ aim, const u16* __restrict__ bre,
                                             const u16* __restrict__ bim, const u16* __restrict__ cre, const u16* __restrict__ cim, const u16* __restrict__ logdt,
                                             const u16* __restrict__ dskip, const u16* __restrict__ wq, const u16* __restrict__ wk, const u16* __restrict__ wv,
                                             const u16* __restrict__ wo, const u16* __restrict__ gw, char* ws, u16* __restrict__ out) {
    extern __shared__ __attribute__((aligned(16))) char smem[];
    const lds_ptr lds = (lds_ptr)smem;
    const unsigned lds0 = (unsigned)(uintptr_t)lds;
    const int lane = threadIdx.x & 63, w = __builtin_amdgcn_readfirstlane(threadIdx.x >> 6);
    const int b = blockIdx.x, cl = (b & 7) * 4 + (b >> 6), g = (b >> 3) & 7;
    const int G = lane >> 4, c16 = lane & 15, r32 = lane & 31, hi = lane >> 5;
    asm volatile("" :: "s"(x), "s"(wq), "s"(wk), "s"(wv), "s"(gw), "s"(ws), "s"(are), "s"(aim), "s"(bre), "s"(bim), "s"(cre), "s"(cim), "s"(logdt), "s"(dskip));
    const lds_u32 ctr = (lds_u32)(lds + L_CTR);
    STAMP(0);
#define MIDBAR ""
    if (w == 11 && lane < C_N) ctr[lane] = 0;
    RAWBAR();
    auto wq_off = [&](int pos) { int c; const int k = wrow_of(pos, lane, c); return (unsigned)(k * ROWB + g * 160 + c * 16); };
    if (__builtin_expect(w < 8, 1)) {
        const int rt = w & 3, kh = w >> 2, rk = g >> 1;
        const bool kvw = rt == rk; const int id6 = ((rt - rk - 1) & 3) + 3 * kh;
        u32x4 xf[10];
        const char* xb = (const char*)x + (size_t)(cl * 64 + rt * 16) * ROWB + kh * 640;
        const unsigned xo = (lane >> 3) * ROWB + (lane & 7) * 16, xo8 = xo + 8 * ROWB;
        if (kvw) {
            __builtin_amdgcn_s_setprio(3);
            const char* wkv = (const char*)((g & 1) ? wv : wk) + kh * 12800; const unsigned d = lds0 + L_WKV + kh * 12800;
            glds64(wkv, lane * 16, d); glds64(wkv, 4096 + lane * 16, d + 4096); glds64(wkv, 8192 + lane * 16, d + 8192); glds16(wkv, 11776 + lane * 16, d + 11776);
        } else { __builtin_amdgcn_s_setprio(APRIO); asm volatile(MIDBAR ::: "memory"); }
        xf[0] = gld16<0>(xb, xo); xf[1] = gld16<0>(xb, xo8); xf[2] = gld16<128>(xb, xo); xf[3] = gld16<128>(xb, xo8); xf[4] = gld16<256>(xb, xo);
        xf[5] = gld16<256>(xb, xo8); xf[6] = gld16<384>(xb, xo); xf[7] = gld16<384>(xb, xo8); xf[8] = gld16<512>(xb, xo); xf[9] = gld16<512>(xb, xo8);
        if (kvw) asm volatile(MIDBAR ::: "memory");
        else if (id6 < 5) {
            unsigned vo = wq_off(id6), t, d = lds0 + L_W + id6 * 1024;
            asm volatile(WPAIR WPAIR WPAIR WPAIR WPAIR "" : "+v"(vo), "=&v"(t), "+s"(d) : "s"(wq) : "memory", "scc");
        } else {
#pragma unroll
            for (int pos = 0; pos < 5; ++pos) { unsigned vo = wq_off(pos) + 5 * 32 * ROWB, t, d = lds0 + L_W + 5 * 5120 + pos * 1024;
                asm volatile(WPAIR "" : "+v"(vo), "=&v"(t), "+s"(d) : "s"(wq) : "memory", "scc"); }
        }
        STAMP(1);
        const lds_ptr sc = lds + (kvw ? L_KVX + kh * 2048 : L_WKV + id6 * 2048);
        const int wx = (lane & 7) ^ (lane >> 4), rx = G ^ (c16 >> 1);
        lds_ptr w0 = sc + (lane >> 3) * 128 + (wx << 4), w1 = sc + 1024 + (lane >> 3) * 128 + ((wx ^ 4) << 4), r0p = sc + c16 * 128 + (rx << 4), r1p = sc + c16 * 128 + ((rx ^ 4) << 4);
        const int r0 = (8 * G + (c16 >> 2)) ^ ((G & 1) << 2);
        lds_ptr p0 = lds + L_W + kh * 51200 + r0 * 160 + (lane & 3) * 8, p1 = lds + L_W + kh * 51200 + (r0 ^ 4) * 160 + (lane & 3) * 8;
        lds_ptr k0 = lds + L_WKV + kh * 12800 + (8 * G + (c16 >> 2)) * 40, k1 = k0 + (lane & 3) * 8;
        PIN4(w0, w1, r0p, r1p); PIN4(k0, k1, p0, p1);
        if (!kvw) await_lo<APRIO>(ctr + C_KVX, 2);
#define XT(i) do { asm volatile("s_cmp_eq_u32 %2, %3\n\ts_cbranch_scc0 1f\n\ts_waitcnt vmcnt(%0)\n1:\n\ts_waitcnt vmcnt(%1)" :: "i"(8 - (i)), "i"(18 - (i)), "s"(rt), "s"(rk) : "memory", "scc"); PIN2(xf[i], xf[(i) + 1]); \
            *(LAS u32x4*)w0 = xf[i]; *(LAS u32x4*)w1 = xf[(i) + 1]; xf[i] = *(const LAS u32x4*)r0p; xf[(i) + 1] = *(const LAS u32x4*)r1p; } while (0)
        XT(0); XT(2); XT(4); XT(6); XT(8);
#undef XT
        STAMP(2);
        if (kvw) {
            f32x4 a0 = {0, 0, 0, 0}, a1 = a0, a2 = a0, a3 = a0;
#define KV(SW) do { u32x4 wa[10], wb[10]; \
            _Pragma("unroll") for (int i = 0; i < 6; ++i) { wa[i] = trfrag(k1, k1 + 160, 1280 * i); wb[i] = trfrag(k0 + 32, k0 + 192, 1280 * i); } \
            _Pragma("unroll") for (int i = 0; i < 10; i += 2) { \
                if (i < 4) { wa[i + 6] = trfrag(k1, k1 + 160, 1280 * (i + 6)); wb[i + 6] = trfrag(k0 + 32, k0 + 192, 1280 * (i + 6)); \
                             wa[i + 7] = trfrag(k1, k1 + 160, 1280 * (i + 7)); wb[i + 7] = trfrag(k0 + 32, k0 + 192, 1280 * (i + 7)); } \
                SBAR(); \
                if (SW) { a0 = MFMA16(xf[i], wa[i], a0); a1 = MFMA16(xf[i], wb[i], a1); a2 = MFMA16(xf[i + 1], wa[i + 1], a2); a3 = MFMA16(xf[i + 1], wb[i + 1], a3); } \
                else { a0 = MFMA16(wa[i], xf[i], a0); a1 = MFMA16(wb[i], xf[i], a1); a2 = MFMA16(wa[i + 1], xf[i + 1], a2); a3 = MFMA16(wb[i + 1], xf[i + 1], a3); } \
                PIN4(a0, a1, a2, a3); SBAR(); } } while (0)
            if (g & 1) KV(1); else KV(0);
#undef KV
            LAS f32x4* kx = (LAS f32x4*)(lds + L_KVX + kh * 2048) + lane;
            kx[0] = a0 + a2; kx[64] = a1 + a3; arrive(ctr + C_KVX, lane);
            __builtin_amdgcn_s_setprio(APRIO);
#pragma unroll
            for (int pos = 0; pos < 5; ++pos) {
                const unsigned d = lds0 + L_W + (6 + 10 * kh) * 5120 + pos * 1024, vs = wq_off(pos) + (6 + 10 * kh) * 32 * ROWB;
                glds16(wq, vs, d); glds16((const char*)wq + 32 * ROWB, vs, d + 5120); glds16((const char*)wq + 64 * ROWB, vs, d + 10240); glds16((const char*)wq + 96 * ROWB, vs, d + 15360);
            }
        } else { arrive(ctr + C_XD, lane); VMWAIT(0); arrive(ctr + C_WQ, lane); }
        STAMP(3);
        f32x4 acc[5];
#pragma unroll
        for (int c = 0; c < 5; ++c) acc[c] = (f32x4){0, 0, 0, 0};
        const int vrow = r32 <= 20 ? r32 : 21;
        const unsigned gb = lds0 + L_RING + kh * GRP_B; unsigned ak = gb + lane * 16, ab = gb + lane * 8, av = gb + (hi * 22 + vrow) * 16;
        unsigned af = lds0 + L_CTR + (C_DONE + (lane & 7)) * 4, ad = lds0 + L_CTR + (C_DONE + w) * 4; const unsigned dst = lds0 + L_RING + w * 1344;
        unsigned go = 2 * SLOT_B + w * 1344 + lane * 16;
        lds_ptr qr0 = lds + L_QP + r32 * QP_ROW + (w & 3) * 80 + hi * 32, qr1 = lds + L_QP + lane * QP_ROW + (w & 3) * 80 + 64, qw = lds + L_QP + kh * QP_B + (rt * 16 + c16) * QP_ROW + 16 * G;
        PIN4(ak, av, qr0, qr1); PIN2(qw, go);
        await_lo<APRIO>(ctr + C_WQ, 7); STAMP(13); gemm_steps<0, 6>(acc, xf, p0, p1);
        if (kvw) { VMWAIT(0); arrive(ctr + C_WQL, lane); }
        arrive(ctr + C_H, lane);
        {
            u32x4 gf[10]; f32x4 ga = {0, 0, 0, 0}, gb = ga;
#pragma unroll
            for (int i = 0; i < 10; ++i) gf[i] = *(const LAS u32x4*)(lds + L_GW + kh * 640 + 64 * i + 16 * G);
            SBAR();
#pragma unroll
            for (int i = 0; i < 10; i += 2) { ga = MFMA16(gf[i], xf[i], ga); gb = MFMA16(gf[i + 1], xf[i + 1], gb); }
            if (G == 0) ((LAS float*)(lds + L_GATE))[kh * 64 + rt * 16 + c16] = ga[0] + gb[0];
        }
        await_lo<APRIO>(ctr + C_WQL, 2); STAMP(4); gemm_steps<6, 10>(acc, xf, p0, p1);
        STAMP(5);
        {
            await_lo<APRIO>(ctr + C_H, 8);
#pragma unroll
            for (int c = 0; c < 5; ++c) *(LAS f32x4*)(qw + 64 * c) = acc[c];
            arrive(ctr + C_QW, lane);
            __builtin_amdgcn_s_setprio(0); await2(ctr + C_QW, 8, ctr + C_READY, 1); __builtin_amdgcn_s_setprio(APRIO);
        }
        const int hd = w & 3;
        u32x4 qa0, qa1; u32x2 qb;
        {
            auto rd4 = [&](lds_ptr p) { const f32x4 a = *(const LAS f32x4*)p + *(const LAS f32x4*)(p + QP_B);
                                        return (u32x2){cvtpk(a[0] * QSCALE, a[1] * QSCALE), cvtpk(a[2] * QSCALE, a[3] * QSCALE)}; };
            const u32x2 a = rd4(qr0), c = rd4(qr0 + 16), d = rd4(qr0 + 32 * QP_ROW), e = rd4(qr0 + 32 * QP_ROW + 16);
            qa0 = (u32x4){a.x, a.y, c.x, c.y}; qa1 = (u32x4){d.x, d.y, e.x, e.y};
            qb = rd4(qr1);
        }
        STAMP(6);
        STAMP(7);
        f32x16 oa = {0}, ob = {0}; unsigned cnt, nd, inc, cap = LSPIN_CAP;
        asm volatile(ATTN_ASM
                     : [oa] "+v"(oa), [ob] "+v"(ob), [cnt] "=&s"(cnt), [nd] "=&s"(nd), [cap] "+s"(cap), [go] "+v"(go), [inc] "=&s"(inc)
                     : [qa0] "v"(qa0), [qa1] "v"(qa1), [qb] "v"(qb), [ak] "v"(ak), [ab] "v"(ab), [av] "v"(av), [af] "v"(af), [ad] "v"(ad), [dst] "s"(dst), [src] "s"(ws + WS_KV), [kh] "s"(kh)
                     : "memory", "scc", "vcc", ATTN_CLOB);
        __builtin_amdgcn_s_setprio(1);
        STAMP(8);
        {
        int lane = threadIdx.x & 63; asm volatile("" : "+v"(lane));
        const int G = lane >> 4, c16 = lane & 15, r32 = lane & 31, hi = lane >> 5;
        const int r0 = (8 * G + (c16 >> 2)) ^ ((G & 1) << 2);
        const lds_ptr p0 = lds + L_W + kh * 51200 + r0 * 160 + (lane & 3) * 8, p1 = lds + L_W + kh * 51200 + (r0 ^ 4) * 160 + (lane & 3) * 8;
        f32x16 o; float l;
        {
            LAS f32x4* xw = (LAS f32x4*)(lds + L_WKV + w * 2688); const LAS f32x4* xr = (const LAS f32x4*)(lds + L_WKV + (w ^ 4) * 2688);
            const f32x16 snd = kh ? oa : ob, own = kh ? ob : oa;
            xw[lane] = snd.s0123; xw[64 + lane] = snd.s4567;
            if (hi == 0) xw[128 + r32] = snd.s89ab; else ((LAS float*)(xw + 160))[r32] = snd[8];
            post(ctr + C_PAIR + w, 1);
            const float lo = __shfl(own[8], r32 + 32);
            await<0>(ctr + C_PAIR + (w ^ 4), 1);
            o.s0123 = own.s0123 + xr[lane]; o.s4567 = own.s4567 + xr[64 + lane]; o.s89ab = own.s89ab + xr[128 + r32];
            l = lo + ((const LAS float*)(xr + 160))[r32];
        }
        bool ok = l >= 0x1p-64f && l <= 0x1p100f;
#pragma unroll
        for (int i = 0; i < 8; ++i) ok = ok && __builtin_fabsf(o[i]) <= 3.0e38f;
#pragma unroll
        for (int i = 8; i < 12; ++i) ok = ok && (hi || __builtin_fabsf(o[i]) <= 3.0e38f);
        if (SLOWOK && __builtin_expect(__any(!ok), 0)) {
            const u32x2 q4 = {(unsigned)__shfl((int)qb.x, kh * 32 + r32), (unsigned)__shfl((int)qb.y, kh * 32 + r32)};
            o = attn_slow(ws + WS_KV, kh ? qa1 : qa0, q4, lane);
            l = __shfl(o[8], r32 + 32);
        }
        {
            const float rl = __builtin_amdgcn_rcpf(l);
            char* op = ws + WS_O + (size_t)cl * 81920;
            const unsigned oo = (kh * 2 + (r32 >> 4)) * 20480 + (r32 & 15) * 16;
            st16_dev(op + (8 * g + 2 * hd) * 256, oo + hi * 256, (u32x4){cvtpk(o[0] * rl, o[1] * rl), cvtpk(o[2] * rl, o[3] * rl), cvtpk(o[4] * rl, o[5] * rl), cvtpk(o[6] * rl, o[7] * rl)});
            if (hi == 0) st8_dev(op + (64 + 2 * g + (hd >> 1)) * 256 + (hd & 1) * 8, oo, (u32x2){cvtpk(o[8] * rl, o[9] * rl), cvtpk(o[10] * rl, o[11] * rl)});
        }
        post(ctr + C_PAIR + w, 2);
        STAMP(9);
        if (kh) __builtin_amdgcn_s_setprio(2);
        const char* ob = ws + WS_O + (size_t)(cl * 4 + rt) * 20480;
        await<0>(ctr + C_WO, 4);
        u32x4 wf[4][5];
#pragma unroll
        for (int c = 0; c < 5; ++c) { wf[0][c] = trfrag(p0, p1, 32 * c); wf[1][c] = trfrag(p0, p1, 5120 + 32 * c); acc[c] = (f32x4){0, 0, 0, 0}; }
#define OLD(i) xf[i] = gld16_dev(ob + OKS(kh, i) * 1024, lane * 16)
        await8<0>(ctr + C_PAIR, 2, lane);
        if (ODELAY) __builtin_amdgcn_s_sleep(ODELAY);
        STAMP(10);
#pragma unroll
        for (int i = 0; i < 10; ++i) OLD(i);
#pragma unroll
        for (int c = 0; c < 5; ++c) { wf[2][c] = trfrag(p0, p1, 10240 + 32 * c); wf[3][c] = trfrag(p0, p1, 15360 + 32 * c); }
        const float gt = 1.f / (1.f + __expf(-(((const LAS float*)(lds + L_GATE))[rt * 16 + c16] + ((const LAS float*)(lds + L_GATE))[64 + rt * 16 + c16]))), gn = 1.f - gt;
        {
#define STALE(i) __any(max(max(xf[i].x, xf[i].y), max(xf[i].z, xf[i].w)) == 0xffffffffu)
#define FRESH(i0, i1) ({ unsigned m_ = 0; _Pragma("unroll") for (int i = (i0); i < (i1); ++i) m_ = max(max(max(m_, xf[i].x), max(xf[i].y, xf[i].z)), xf[i].w); !__any(m_ == 0xffffffffu); })
#define REFETCH(i0, i1) _Pragma("unroll 1") for (unsigned n = 0; n < SPIN_CAP && !FRESH(i0, i1); ++n) { \
                VMWAIT(0); _Pragma("unroll") for (int i = (i0); i < 10; ++i) asm volatile("" : "+v"(xf[i])); \
                __builtin_amdgcn_s_sleep(OSLEEP); \
                _Pragma("unroll") for (int i = (i0); i < 10; ++i) asm volatile("v_max3_u32 %1, %2, %3, %4\n\tv_max_u32 %1, %1, %5\n\tv_cmp_eq_u32 vcc, -1, %1\n\ts_cbranch_vccz 1f\n\tglobal_load_dwordx4 %0, %6, %7 sc1\n1:" \
                    : "+v"(xf[i]), "=&v"(tmp) : "v"(xf[i].x), "v"(xf[i].y), "v"(xf[i].z), "v"(xf[i].w), "v"(lane * 16), "s"(ob + OKS(kh, i) * 1024) : "memory", "vcc"); \
                VMWAIT(0); _Pragma("unroll") for (int i = (i0); i < 10; ++i) asm volatile("" : "+v"(xf[i])); }
            unsigned tmp;
#define STAGE(i0, i1) do { VMWAIT(10 - (i1)); _Pragma("unroll") for (int i = (i0); i < (i1); ++i) asm volatile("" : "+v"(xf[i])); REFETCH(i0, i1); gemm_run<i0, i1>(acc, xf, wf, p0, p1, sh); } while (0)
            const int sh = kh ? L_WO3 - 76800 : 0;
            STAGE(0, 3);
            STAMP(12);
            STAGE(3, 6); STAGE(6, 10);
#undef STAGE
#undef REFETCH
#undef FRESH
#undef OLD
#undef STALE
        }
        await<0>(ctr + C_AT, 1);
        const lds_ptr ap = lds + L_AT + (4 * G + (c16 >> 2)) * 128 + rt * 32 + (lane & 3) * 8;
        u32x2 at[3];
        at[0] = trrd(ap + (kh ? 3 : 0) * 2048); at[1] = trrd(ap + (kh ? 4 : 1) * 2048); at[2] = trrd(ap + 4096);
        STAMP(11);
        {
            LAS f32x4* xw = (LAS f32x4*)(lds + L_RING + w * 2688) + lane; const LAS f32x4* xr = (const LAS f32x4*)(lds + L_RING + (w ^ 4) * 2688) + lane;
            if (kh) { xw[0] = acc[0]; xw[64] = acc[1]; ((LAS f32x2*)(xw - lane + 128))[lane] = acc[2].xy; }
            else { xw[0] = acc[3]; xw[64] = acc[4]; ((LAS f32x2*)(xw - lane + 128))[lane] = acc[2].zw; }
            post(ctr + C_PAIR + w, 3);
            char* ob2 = (char*)out + (size_t)(cl * 64 + rt * 16 + c16) * ROWB + g * 160 + 8 * G;
            auto fin = [&](int c, u32x2 a, const f32x4& s) {
                OST8(ob2 + 32 * c) (u32x2){cvtpk(gt * bf_lo(a.x) + gn * s[0], gt * bf_hi(a.x) + gn * s[1]), cvtpk(gt * bf_lo(a.y) + gn * s[2], gt * bf_hi(a.y) + gn * s[3])} OSTE;
            };
            await<0>(ctr + C_PAIR + (w ^ 4), 3);
            const f32x2 h2 = ((const LAS f32x2*)(xr - lane + 128))[lane];
            if (kh) { fin(3, at[0], acc[3] + xr[0]); fin(4, at[1], acc[4] + xr[64]);
                      OST4(ob2 + 68) cvtpk(gt * bf_lo(at[2].y) + gn * (acc[2][2] + h2.x), gt * bf_hi(at[2].y) + gn * (acc[2][3] + h2.y)) OSTE; }
            else { fin(0, at[0], acc[0] + xr[0]); fin(1, at[1], acc[1] + xr[64]);
                   OST4(ob2 + 64) cvtpk(gt * bf_lo(at[2].x) + gn * (acc[2][0] + h2.x), gt * bf_hi(at[2].x) + gn * (acc[2][1] + h2.y)) OSTE; }
        }
        if (b == 0 && w == 0) { const unsigned tag = await(ctr + C_TAG, 1); if (lane == 0) st4_dev(ws, WS_EPOCH, tag); }
        }
    } else {
        const int xw = w - 8;
        unsigned tag, wab[5], wcc[5];
#define WOF() _Pragma("unroll") for (int i = 0; i < 5; ++i) { int c; const int r = wrow_of(i, lane, c); wab[i] = wo_row_ab(r) * ROWB + g * 160 + c * 16; wcc[i] = wo_row_c(r) * ROWB + g * 160 + c * 16; PIN2(wab[i], wcc[i]); }
#define WO_SLOT(h, i, C) do { const unsigned d_ = lds0 + (h) * 51200 + (i) * 5120 + ((h) && (i) >= 5 ? L_WO3 - 76800 : 0); \
            const char* src_ = (const char*)wo + ((C) ? (2 * (h) + (i) - 8) * 160 : (8 * (h) + (((i) + g) & 7)) * 40) * ROWB; \
            _Pragma("unroll") for (int p = 0; p < 5; ++p) glds16(src_, (C) ? wcc[p] : wab[p], d_ + p * 1024); } while (0)
#define GLD4(d, p, off) asm volatile("global_load_dword %0, %1, %2" : "=v"(d) : "v"(off), "s"(p) : "memory")
#define GLD2(d, p, off) asm volatile("global_load_ushort %0, %1, %2" : "=v"(d) : "v"(off), "s"(p) : "memory")
        if (xw == 3) {
            __builtin_amdgcn_s_setprio(3);
            unsigned ep; asm volatile("global_load_dword %0, %1, %2 sc1" : "=v"(ep) : "v"(0), "s"(ws) : "memory");
#pragma unroll
            for (int p = 0; p < 2; ++p) glds16(gw, p * 256 + lane * 16, lds0 + L_GW + p * 256);
            VMWAIT(0); asm volatile("" : "+v"(ep));
            arrive(ctr + C_WQ, lane);
            tag = (unsigned)__builtin_amdgcn_readfirstlane(ep) + 0x9e3779b9u;
            if (tag == 0) tag = 0x9e3779b9u;
            post(ctr + C_TAG, tag);
            STAMP(1);
            const int rk = g >> 1;
            char* kvb = ws + WS_KV + cl * GRP_B;
            const unsigned vo1 = (g & 1) ? 2560 + rk * VS_B + (G & 1) * 352 + 8 * (G >> 1) + c16 * 16 : (rk >> 1) * 1024 + ((G >> 1) * 32 + (rk & 1) * 16 + c16) * 16 + (G & 1) * 8;
            const unsigned vo2 = (g & 1) ? vo1 + 256 : 2048 + (16 * rk + c16) * 8;
            await<0>(ctr + C_KVX, 2);
            const LAS f32x4* kx = (const LAS f32x4*)(lds + L_KVX) + lane;
            const f32x4 a0 = kx[0] + kx[128], a1 = kx[64] + kx[192];
            u32x2 v1 = {cvtpk(a1[0], a1[1]), cvtpk(a1[2], a1[3])};
            st8_dev(kvb, vo1, (u32x2){cvtpk(a0[0], a0[1]), cvtpk(a0[2], a0[3])});
            if ((g & 1) && c16 == 4) v1 = (u32x2){0x3f803f80u, 0x3f803f80u};
            if ((g & 1) && c16 == 5) v1 = (u32x2){0u, 0u};
            if ((g & 1) ? c16 < 6 : G == 0) st8_dev(kvb, vo2, v1);
            STAMP(2);
            VMWAIT(0);

            if (lane == 0) st4_dev(ws, WS_FKV + (cl * 8 + g) * FL, tag);
            STAMP(9);
            {
                const char* xs = (const char*)x + (size_t)cl * 64 * ROWB + g * 160;
#pragma unroll
                for (int i = 0; i < 4; ++i) { glds16(xs + i * 16 * ROWB, (lane >> 2) * ROWB + (lane & 3) * 16, lds0 + L_US0 + i * 1024);
                                              glds16(xs + i * 16 * ROWB + 64, (lane >> 2) * ROWB + (lane & 3) * 16, lds0 + L_US1 + i * 1024); }
#pragma unroll
                for (int i = 0; i < 2; ++i) glds16(xs + i * 32 * ROWB + 128, (lane >> 1) * ROWB + (lane & 1) * 16, lds0 + L_US2 + i * 1024);
            }
            poll4(ws + WS_FKV, r32 * FL, tag);
            STAMP(10);
            await<0>(ctr + C_XD, 6);
#pragma unroll
            for (int c = 0; c < 2; ++c) { const char* src = ws + WS_KV + c * SLOT_B; const unsigned d = lds0 + L_RING + (c + 2) * SLOT_B;
                                          glds64_dev(src, lane * 16, d); glds64_dev(src, 4096 + lane * 16, d + 4096); glds64_dev(src, 6656 + lane * 16, d + 6656); }
#pragma unroll 1
            for (unsigned n = 0; n < SPIN_CAP; ++n) {
                u32x4 v;
                asm volatile("global_load_dword %0, %4, %6 sc1\n\tglobal_load_dword %1, %5, %6 sc1\n\tglobal_load_dword %2, %4, %7 sc1\n\tglobal_load_dword %3, %5, %7 sc1\n\ts_waitcnt vmcnt(0)"
                             : "=&v"(v.x), "=&v"(v.y), "=&v"(v.z), "=&v"(v.w) : "v"(lane * FL), "v"((64 + lane) * FL), "s"(ws + WS_FKV), "s"(ws + WS_FKV + 128 * FL) : "memory");
                if (__all(v.x == tag && v.y == tag && v.z == tag && v.w == tag)) break;
                __builtin_amdgcn_s_sleep(2);
            }
            post(ctr + C_READY, 1);
            STAMP(11);
            u32x4 ut[10];
            {
                const int ur = 8 * G + (c16 >> 2);
#pragma unroll
                for (int i = 0; i < 2; ++i)
#pragma unroll
                    for (int t = 0; t < 5; ++t) { const int rs = t < 4 ? 64 : 32; const lds_ptr p = lds + (t < 2 ? L_US0 : t < 4 ? L_US1 : L_US2) + (32 * i + ur) * rs + (t < 4 ? (t & 1) * 32 : 0) + (lane & 3) * 8;
                                                  ut[5 * i + t] = trfrag(p, p + 4 * rs, 0); }
            }
#pragma unroll
            for (int i = 0; i < 2; ++i)
#pragma unroll
                for (int t = 0; t < 5; ++t) st16_dev(ws + WS_UT + (size_t)(80 * g + 16 * t) * UT_B + cl * 128 + 64 * i, c16 * UT_B + 16 * G, ut[5 * i + t]);
#pragma unroll
            for (int i = 0; i < 10; ++i)
                st16_dev(ws + WS_O + (size_t)cl * 81920 + (i < 8 ? (i >> 1) * 20480 + 2048 * g + (i & 1) * 1024 : (i - 8) * 40960 + 16384 + 512 * g),
                         i < 8 ? lane * 16 : hi * 20480 + r32 * 16, (u32x4){~0u, ~0u, ~0u, ~0u});
            STAMP(7);
            WOF();
            VMWAIT(0);
            if (lane == 0) st4_dev(ws, WS_FU + (g * 32 + cl) * FL, tag);
            STAMP(8);
            unsigned pf[4];
#pragma unroll
            for (int i = 0; i < 4; ++i) GLD4(pf[i], wo, ((b >> 3) * 200 + min(64 * i + lane, 199)) * 128);
            VMWAIT(0); PIN4(pf[0], pf[1], pf[2], pf[3]);
        } else {
            const int ch = xw < 2 ? b + 256 * xw : 512 + (b & 127);
            unsigned pw[6], pl, pd, pf[2];
            const unsigned o = (ch * 64 + lane) * 4;
            __builtin_amdgcn_s_setprio(PPRIO);
            GLD2(pl, logdt, ch * 2); GLD4(pw[0], are, o); GLD4(pw[1], aim, o); GLD4(pw[2], bre, o); GLD4(pw[3], bim, o); GLD4(pw[4], cre, o); GLD4(pw[5], cim, o); GLD2(pd, dskip, ch * 2);
            __builtin_amdgcn_s_setprio(PFPRIO);
            const int sh = b >> 3;
            const void* pb = xw < 2 ? (const void*)wq : (const void*)x;
            const unsigned po = (xw < 2 ? sh * 200 + xw * 100 : (b & 7) * 2560 + sh * 80) + lane;
            GLD4(pf[0], logdt, 0); GLD4(pf[1], logdt, 0);
            __builtin_amdgcn_s_setprio(KPRIO);
            VMWAIT(2); PIN4(pw[0], pw[1], pw[2], pw[3]); PIN4(pw[4], pw[5], pl, pd);
            STAMP(7);
            const f32x16 kacc = kgen_wave(lane, lds + L_TAB + xw * TAB_B, pw[0], pw[1], pw[2], pw[3], pw[4], pw[5], pl, pd, [&]() { __builtin_amdgcn_s_setprio(KPRIO2); });
            STAMP(1);
            kexpand(kacc, lane, lds + L_TAB + xw * TAB_B);
            WOF();
            VMWAIT(0); PIN2(pf[0], pf[1]);
            __builtin_amdgcn_s_setprio(3);
            tag = await(ctr + C_TAG, 1);
            poll4(ws + WS_FU, ((ch / 80) * 32 + r32) * FL, tag);
            STAMP(2);
            await<1>(ctr + C_QW, 8);
            glds64_dev(ws + WS_UT + (size_t)ch * UT_B, lane * 16, lds0 + L_U + xw * U_B + 512);
            if (lane < 32) *(LAS u32x4*)(lds + L_U + xw * U_B + lane * 16) = (u32x4){0, 0, 0, 0};
            VMWAIT(0);
            post(ctr + C_BUF + xw, 1);
        }
#undef GLD4
#undef GLD2
        STAMP(3);
        await8<2>(ctr + C_DONE, 1, lane);
        WO_SLOT(0, xw, 0); WO_SLOT(1, 1 + xw, 0);
        if (xw == 2) WO_SLOT(0, 4, 0);
        __builtin_amdgcn_s_setprio(CVPRIO);
        {
            const int fo = L_TAB + ((c16 & 1) ? 0 : 4160) + (1015 - (c16 >> 1) + 4 * G) * 4 - 3584, go = L_U + 512 - 32 + c16 * 32 + G * 16;
            f32x4 y[8];
            const int njob = xw == 3 ? 2 : 1;
#pragma unroll 1
            for (int job = 0; job < njob; ++job) {
                const int bf = xw == 0 ? 0 : xw == 3 ? job : 1, q0 = xw < 2 ? 0 : 5 + job, q1 = xw < 2 ? 5 : xw == 2 ? 6 : 8;
#pragma unroll
                for (int i = 0; i < 8; ++i) y[i] = (f32x4){0, 0, 0, 0};
                LAS f32x4* pp = (LAS f32x4*)(lds + L_CP + bf * 8192) + lane;
                if (job == 1) {
                    await<2>(ctr + C_PART + 1, 1);
#pragma unroll
                    for (int i = 0; i < 8; ++i) y[i] = pp[64 * i];
                }
                await<2>(ctr + C_BUF + bf, 1);
#pragma unroll 1
                for (int rho = q0; rho < q1; ++rho) conv_class<0xff>(y, lds + bf * TAB_B + fo - 64 * rho, lds + bf * U_B + go - 64 * rho);
                if (xw >= 2) {
#pragma unroll
                    for (int i = 0; i < 8; ++i) pp[64 * i] = y[i];
                    post(ctr + C_PART + bf, 1 + job);
                }
            }
            if (xw < 3) {
                int tiles = 0xff;
                if (xw == 2) {
#pragma unroll
                    for (int i = 0; i < 8; ++i) y[i] = (f32x4){0, 0, 0, 0};
                    tiles = b < 128 ? 0xc3 : 0x3c;
                    const lds_ptr fa = lds + 2 * TAB_B + fo, ga = lds + 2 * U_B + go;
                    if (b < 128) {
#pragma unroll 1
                        for (int rho = 0; rho < 8; ++rho) conv_class<0xc3>(y, fa - 64 * rho, ga - 64 * rho);
                    } else {
#pragma unroll 1
                        for (int rho = 0; rho < 8; ++rho) conv_class<0x3c>(y, fa - 64 * rho, ga - 64 * rho);
                    }
                } else {
                    await<2>(ctr + C_PART + xw, 1 + xw);
#pragma unroll
                    for (int i = 0; i < 8; ++i) y[i] += ((const LAS f32x4*)(lds + L_CP + xw * 8192))[64 * i + lane];
                }
                const int ch = xw < 2 ? b + 256 * xw : 512 + (b & 127);
                char* ap = ws + WS_AT + (size_t)ch * (SEQ * 2);
#pragma unroll
                for (int i = 0; i < 8; ++i) if ((tiles >> i) & 1) st8_dev(ap, 512 * i + c16 * 32 + G * 8, (u32x2){cvtpk(y[i][0], y[i][1]), cvtpk(y[i][2], y[i][3])});
                VMWAIT(0);
                if (lane == 0) st4_dev(ws, WS_FA + (xw < 2 ? ch : 512 + b) * FL, tag);
            }
        }
        STAMP(4);
        __builtin_amdgcn_s_setprio(3);
        arrive(ctr + C_CONV, lane);
        await<4>(ctr + C_CONV, 4);
        if (xw < 3) {
            WO_SLOT(0, 5 + xw, 0); WO_SLOT(1, 5 + xw, 0);
            if (xw < 2) { WO_SLOT(0, 8 + xw, 1); WO_SLOT(1, 8 + xw, 1); } else WO_SLOT(1, 0, 0);
            VMWAIT(0);
            arrive(ctr + C_WO, lane);
        } else {
            poll4(ws + WS_FU, ((lane & 7) * 32 + cl) * FL, tag);
            arrive(ctr + C_WO, lane);
            const int hs = ((0x3c >> (cl >> 2)) & 1) * 128, c0 = 80 * g + lane, c1 = c0 + 16;
#pragma unroll 1
            for (unsigned n = 0; n < SPIN_CAP; ++n) {
                u32x2 v;
                asm volatile("global_load_dword %0, %2, %4 sc1\n\tglobal_load_dword %1, %3, %4 sc1\n\ts_waitcnt vmcnt(0)"
                             : "=&v"(v.x), "=&v"(v.y) : "v"((c0 + (c0 >= 512 ? hs : 0)) * FL), "v"((c1 + (c1 >= 512 ? hs : 0)) * FL), "s"(ws + WS_FA) : "memory");
                if (__all(v.x == tag && v.y == tag)) break;
                __builtin_amdgcn_s_sleep(3);
            }
#pragma unroll
            for (int pc = 0; pc < 10; ++pc)
                glds16_dev(ws + WS_AT, (80 * g + 8 * pc + (lane >> 3)) * (SEQ * 2) + cl * 128 + (lane & 7) * 16, lds0 + L_AT + pc * 1024);
            VMWAIT(0);
            post(ctr + C_AT, 1);
        }
        STAMP(5);
        STAMP(6);
    }
}

extern "C" void kernel_launch(void* const* d_in, const int* in_sizes, int n_in, void* d_out, int out_size, void* d_ws, size_t ws_size, hipStream_t stream) {
    (void)in_sizes; (void)n_in; (void)out_size; (void)ws_size;
    const u16* const* in = (const u16* const*)d_in;
    (void)hipFuncSetAttribute((const void*)fused, hipFuncAttributeMaxDynamicSharedMemorySize, L_END);
    fused<<<256, 768, L_END, stream>>>(in[0], in[1], in[2], in[3], in[4], in[5], in[6], in[7], in[8], in[10], in[11], in[12], in[13], in[14], (char*)d_ws, (u16*)d_out);
}
```
